```python
import torch

def _softmax(x, axis=-1):
    return torch.softmax(x, dim=axis)
def _gelu(x):
    return torch.nn.functional.gelu(x, approximate="tanh")
def _silu(x):
    return torch.nn.functional.silu(x)
def _sigmoid(x):
    return torch.sigmoid(x)
def _layernorm(x, axis=-1, eps=1e-5):
    m = x.mean(dim=axis, keepdim=True)
    v = x.var(dim=axis, keepdim=True, unbiased=False)
    return (x - m) / torch.sqrt(v + eps)
def _scatter_add(tgt, idx, src, axis=0):
    return tgt.clone().index_add_(axis, idx, src)
def _segment_sum(src, seg, n):
    out = torch.zeros((n,) + src.shape[1:], dtype=src.dtype)
    return out.index_add_(0, seg, src)
def _pad(x, pw):
    flat = tuple(v for pair in reversed(pw) for v in pair)
    return torch.nn.functional.pad(x, flat)

kernel_name = 'parallel_sum__grouped_kv_mqa__glu_gated_reglu__prompt'

def setup_inputs():

    _w = (lambda a: (lambda t: t.to(torch.bfloat16) if t.is_floating_point() else t)(torch.as_tensor(a)))
    def _A_setup():
        B, S, D, H = 1, 2048, 640, 32
        Dh = D // H
        return {
            "x": _w((lambda *s: torch.randn(*s, dtype=torch.bfloat16) * 0.05)(B, S, D)),
            "wq": _w((lambda *s: torch.randn(*s, dtype=torch.bfloat16) * 0.05)(D, H * Dh)),
            "wk": _w((lambda *s: torch.randn(*s, dtype=torch.bfloat16) * 0.05)(D, Dh)),
            "wv": _w((lambda *s: torch.randn(*s, dtype=torch.bfloat16) * 0.05)(D, Dh)),
            "wo": _w((lambda *s: torch.randn(*s, dtype=torch.bfloat16) * 0.05)(H * Dh, D)),
        }
    _A_in = _A_setup()

    def _B_setup():
        B, S, D, F = 1, 2048, 640, 1728
        return {
            "x": _w((lambda *s: torch.randn(*s, dtype=torch.bfloat16) * 0.05)(B, S, D)),
            "W_gate": _w((lambda *s: torch.randn(*s, dtype=torch.bfloat16) * 0.8)(D, F) * (2.0 / D) ** 0.5),
            "W_up": _w((lambda *s: torch.randn(*s, dtype=torch.bfloat16) * 0.8)(D, F) * (2.0 / D) ** 0.5),
            "W_down": _w((lambda *s: torch.randn(*s, dtype=torch.bfloat16) * 0.8)(F, D) * (1.0 / F) ** 0.5),
        }
    _B_in = _B_setup()

    return {
        "A_x": _A_in["x"],
        "A_wq": _A_in["wq"],
        "A_wk": _A_in["wk"],
        "A_wv": _A_in["wv"],
        "A_wo": _A_in["wo"],
        "B_x": _B_in["x"],
        "B_W_gate": _B_in["W_gate"],
        "B_W_up": _B_in["W_up"],
        "B_W_down": _B_in["W_down"],
    }

def reference(A_x, A_wq, A_wk, A_wv, A_wo, B_x, B_W_gate, B_W_up, B_W_down):
    def _A_ref(x, wq, wk, wv, wo):
        B, S, D = x.shape
        H = 32
        Dh = D // H
        q = (lambda x, *d: x.permute(*d))((x @ wq).reshape(B, S, H, Dh), 0, 2, 1, 3)
        k = x @ wk
        v = x @ wv
        scores = torch.einsum("bhqd,bkd->bhqk", q, k) * (Dh ** -0.5)
        p = _softmax(scores, axis=-1)
        o = torch.einsum("bhqk,bkd->bhqd", p, v)
        return (lambda x, *d: x.permute(*d))(o, 0, 2, 1, 3).reshape(B, S, H * Dh) @ wo

    def _B_ref(x, W_gate, W_up, W_down):
        gate = torch.einsum("bsd,df->bsf", x, W_gate)
        up = torch.einsum("bsd,df->bsf", x, W_up)
        gate = gate * (lambda x, dt: x.to(dt))(gate > 0, gate.dtype)
        h = gate * up
        return torch.einsum("bsf,fd->bsd", h, W_down)

    x = A_x
    xn = _layernorm(x, axis=-1)
    return x + _A_ref(xn, A_wq, A_wk, A_wv, A_wo) + _B_ref(xn, B_W_gate, B_W_up, B_W_down)
```

```cpp
#include <hip/hip_runtime.h>
#include <hip/hip_bf16.h>
#include <stdint.h>

typedef __attribute__((ext_vector_type(8))) __bf16 bf16x8;
typedef __attribute__((ext_vector_type(16))) __bf16 bf16x16;
typedef __attribute__((ext_vector_type(4))) short s16x4;
typedef __attribute__((ext_vector_type(16))) float f32x16;
typedef __attribute__((ext_vector_type(8))) float f32x8;
typedef __attribute__((ext_vector_type(4))) float f32x4;
typedef __attribute__((ext_vector_type(2))) float f32x2;
typedef __attribute__((ext_vector_type(4))) unsigned u32x4;
typedef __attribute__((ext_vector_type(2))) unsigned u32x2;
typedef __attribute__((ext_vector_type(2))) __bf16 bf16x2;
#define LAS __attribute__((address_space(3)))
#define DEV __device__ __forceinline__
#define SBAR() __builtin_amdgcn_sched_barrier(0)
#define PIN(x) asm volatile("" : "+v"(x))

constexpr int S_ = 2048, D_ = 640, F_ = 1728;
constexpr int NSTEP_H = F_ / 32, NSTEP_O = D_ / 32, NSTEP = NSTEP_H + NSTEP_O;
constexpr float QSCALE = 0.22360679774997896f * 1.4426950408889634f;
constexpr size_t WS_STATS = 0;
constexpr size_t WS_KMAX = 16384;
constexpr size_t WS_KV = 32768;
constexpr size_t WS_Q = WS_KV + 32 * 7168;
constexpr size_t WS_ACT = WS_Q + (size_t)S_ * D_ * 2 + 256;

DEV unsigned pk_bf16(float lo, float hi) { bf16x2 v = {(__bf16)lo, (__bf16)hi}; return __builtin_bit_cast(unsigned, v); }
DEV float bf_lo(unsigned u) { return __builtin_bit_cast(float, u << 16); }
DEV float bf_hi(unsigned u) { return __builtin_bit_cast(float, u & 0xffff0000u); }
DEV float swap32_add(float v) {
  auto r = __builtin_amdgcn_permlane32_swap(__builtin_bit_cast(unsigned, v), __builtin_bit_cast(unsigned, v), false, false);
  return __builtin_bit_cast(float, r[0]) + __builtin_bit_cast(float, r[1]); }
DEV float swap32_max(float v) {
  auto r = __builtin_amdgcn_permlane32_swap(__builtin_bit_cast(unsigned, v), __builtin_bit_cast(unsigned, v), false, false);
  return fmaxf(__builtin_bit_cast(float, r[0]), __builtin_bit_cast(float, r[1])); }
DEV void glds16(const void* sbase, unsigned voff, unsigned lds_base) {
  unsigned sv;
  asm volatile("s_mov_b32 %0, m0\n\ts_mov_b32 m0, %3\n\ts_nop 0\n\tglobal_load_lds_dwordx4 %1, %2\n\ts_mov_b32 m0, %0"
               : "=&s"(sv) : "v"(voff), "s"(sbase), "s"(lds_base) : "memory"); }
DEV void glds16_20(const void* sbase, unsigned voff, unsigned lds_base) {
  unsigned sv;
  asm volatile("s_mov_b32 %0, m0\n\ts_mov_b32 m0, %3\n\ts_mov_b64 exec, 0xfffff\n\tglobal_load_lds_dwordx4 %1, %2\n\ts_mov_b64 exec, -1\n\ts_mov_b32 m0, %0"
               : "=&s"(sv) : "v"(voff), "s"(sbase), "s"(lds_base) : "memory"); }
DEV bf16x8 lds_rd128(unsigned a) { return *(const LAS bf16x8*)a; }
DEV s16x4 lds_tr(unsigned a) { return __builtin_amdgcn_ds_read_tr16_b64_v4i16((LAS s16x4*)a); }
DEV bf16x8 cat8(s16x4 a, s16x4 b) { return __builtin_bit_cast(bf16x8, __builtin_shufflevector(a, b, 0, 1, 2, 3, 4, 5, 6, 7)); }
#define MFMA32(a, b, c) __builtin_amdgcn_mfma_f32_32x32x16_bf16(a, b, c, 0, 0, 0)
#define MFMA16(a, b, c) __builtin_amdgcn_mfma_f32_16x16x32_bf16(a, b, c, 0, 0, 0)

__global__ __launch_bounds__(512) void k_stats(const unsigned* __restrict__ x, f32x2* __restrict__ stats) {
  const int lane = threadIdx.x & 63, row = blockIdx.x * 8 + (threadIdx.x >> 6);
  unsigned v[5];
#pragma unroll
  for (int i = 0; i < 5; ++i) v[i] = x[row * 320 + i * 64 + lane];
  float s = 0.f;
#pragma unroll
  for (int i = 0; i < 5; ++i) s += bf_lo(v[i]) + bf_hi(v[i]);
#pragma unroll
  for (int o = 32; o; o >>= 1) s += __shfl_xor(s, o);
  const float m = s * (1.f / D_);
  float q = 0.f;
#pragma unroll
  for (int i = 0; i < 5; ++i) { float a = bf_lo(v[i]) - m, b = bf_hi(v[i]) - m; q += a * a + b * b; }
#pragma unroll
  for (int o = 32; o; o >>= 1) q += __shfl_xor(q, o);
  if (lane == 0) stats[row] = f32x2{m, 1.f / sqrtf(q * (1.f / D_) + 1e-5f)};
}

constexpr int G1_XT = 32768, G1_WT = 16384, G1_KT = 3072, G1_STG = G1_XT + G1_WT + G1_KT;
struct G1P { const char* x; const char* wq; const char* wk; const char* wv; const char* wg; const char* wu; char* ws; };

DEV float sum8(bf16x8 f) { u32x4 u = __builtin_bit_cast(u32x4, f);
  return ((bf_lo(u[0]) + bf_hi(u[0])) + (bf_lo(u[1]) + bf_hi(u[1]))) + ((bf_lo(u[2]) + bf_hi(u[2])) + (bf_lo(u[3]) + bf_hi(u[3]))); }

template <int MODE>
DEV void gemm1_body(const G1P& p, unsigned lds0, int rb, int cb, int w, int lane) {
  constexpr bool ISQ = MODE != 0;
  const int fh = w >> 2, tg = w & 3, l31 = lane & 31, hh = lane >> 5;
  const int sw = ISQ ? (cb & 1) : 0;
  const int row0 = rb * 256;
  const unsigned xoff0 = (unsigned)((row0 + 32 * (w ^ sw) + (lane >> 3)) * 1280 + 16 * ((lane & 7) ^ (lane >> 4)));
  const unsigned xoff1 = (unsigned)((row0 + 32 * (w ^ sw) + 8 + (lane >> 3)) * 1280 + 16 * ((lane & 7) ^ ((lane >> 4) | 4)));
  constexpr int LDW = ISQ ? 1280 : 3456;
  const unsigned woff = (unsigned)((8 * w + ((lane >> 2) & 7)) * LDW + 64 * hh + 16 * ((lane & 3) ^ ((2 * w + ((lane >> 4) & 1)) & 3)));
  const char* wA = ISQ ? p.wq + cb * 256 : p.wg + (cb - 5) * 128;
  const char* wB = ISQ ? p.wq + cb * 256 + 128 : p.wu + (cb - 5) * 128;
  const char* wKV = (cb & 2) ? p.wv : p.wk;
  const bool haskv = ISQ && cb < 4;
  auto stage = [&](int kt, unsigned sb) {
    const char* xs = p.x + kt * 128;
    glds16(xs, xoff0, sb + w * 4096);
    glds16(xs, xoff1, sb + w * 4096 + 1024);
    glds16(xs + 16 * 1280, xoff0, sb + w * 4096 + 2048);
    glds16(xs + 16 * 1280, xoff1, sb + w * 4096 + 3072);
    glds16(wA + kt * 64 * LDW, woff, sb + G1_XT + w * 2048);
    glds16(wB + kt * 64 * LDW, woff, sb + G1_XT + w * 2048 + 1024);
    if (ISQ) glds16_20(wKV + kt * 2560 + w * 320, lane * 16, sb + G1_XT + G1_WT + w * 320);
  };
  stage(0, lds0); stage(1, lds0 + G1_STG);
  unsigned xa[4];
#pragma unroll
  for (int s = 0; s < 4; ++s) xa[s] = (64 * tg + l31) * 128 + 16 * ((2 * s + hh) ^ ((l31 >> 1) & 7));
  const int blk = (lane >> 4) & 1, q4 = (lane & 15) >> 2, p4 = lane & 3;
  constexpr int T1OFF = ISQ ? 512 : 1024;
  unsigned wa[2];
#pragma unroll
  for (int t = 0; t < 2; ++t)
    wa[t] = G1_XT + 2048 * hh + (ISQ ? 1024 : 512) * fh + 64 * (4 * t + q4) + 16 * ((2 * blk + (p4 >> 1)) ^ (2 * hh + t)) + 8 * (p4 & 1);
  const unsigned ka = G1_XT + G1_WT + 40 * (8 * hh + q4) + 32 * blk + 8 * p4;

  f32x16 acc[2][2] = {}, acckv = {};
  float cs0 = 0.f, cs1 = 0.f, cskv = 0.f;
  unsigned sb = lds0, sbn = lds0 + 2 * G1_STG;
  for (int kt = 0; kt < 10; ++kt) {
    if (kt < 9) { if (ISQ) asm volatile("s_waitcnt vmcnt(7) lgkmcnt(0)\n\ts_barrier" ::: "memory"); else asm volatile("s_waitcnt vmcnt(6) lgkmcnt(0)\n\ts_barrier" ::: "memory"); }
    else asm volatile("s_waitcnt vmcnt(0) lgkmcnt(0)\n\ts_barrier" ::: "memory");
    if (kt < 8) stage(kt + 2, sbn);
    const bool docs = ((kt + 2 * fh) & 3) == tg;
    auto comp = [&](auto CS) {
#pragma unroll
      for (int s = 0; s < 4; ++s) {
        bf16x8 b0 = lds_rd128(sb + xa[s]), b1 = lds_rd128(sb + xa[s] + 4096);
        bf16x8 a0 = cat8(lds_tr(sb + wa[0] + 4096 * s), lds_tr(sb + wa[1] + 4096 * s));
        bf16x8 a1 = cat8(lds_tr(sb + wa[0] + 4096 * s + T1OFF), lds_tr(sb + wa[1] + 4096 * s + T1OFF));
        acc[0][0] = MFMA32(a0, b0, acc[0][0]);
        acc[0][1] = MFMA32(a0, b1, acc[0][1]);
        acc[1][0] = MFMA32(a1, b0, acc[1][0]);
        acc[1][1] = MFMA32(a1, b1, acc[1][1]);
        if (MODE >= 2) {
          bf16x8 ak = cat8(lds_tr(sb + ka + 640 * s), lds_tr(sb + ka + 640 * s + 160));
          if (MODE == 2) acckv = MFMA32(ak, b0, acckv); else acckv = MFMA32(b0, ak, acckv);
          cskv += sum8(ak);
        }
        if (CS.value) { cs0 += sum8(a0); cs1 += sum8(a1); }
      }
    };
    if (docs) comp(std::integral_constant<bool, true>{}); else comp(std::integral_constant<bool, false>{});
    sbn = sb; sb = (sb == lds0 + 2 * G1_STG) ? lds0 : sb + G1_STG;
  }
  const unsigned csb = lds0 + G1_STG;
  *(LAS float*)(csb + (((fh * 2 + 0) * 4 + tg) * 64 + lane) * 4) = cs0;
  *(LAS float*)(csb + (((fh * 2 + 1) * 4 + tg) * 64 + lane) * 4) = cs1;
  const f32x2* stats = (const f32x2*)(p.ws + WS_STATS);
  const int tok0 = row0 + 64 * tg;
  f32x2 st[2];
#pragma unroll
  for (int n = 0; n < 2; ++n) st[n] = stats[tok0 + 32 * (n ^ sw) + l31];
  asm volatile("s_waitcnt lgkmcnt(0)\n\ts_barrier" ::: "memory");
  {
    float c = 0.f;
#pragma unroll
    for (int g = 0; g < 4; ++g) {
      const unsigned a = csb + (((fh * 2 + hh) * 4 + g) * 64 + l31) * 4;
      c += *(const LAS float*)a + *(const LAS float*)(a + 128);
    }
    *(LAS float*)(csb + 4096 + w * 512 + lane * 4) = c;
    if (MODE >= 2) { cskv = swap32_add(cskv); *(LAS float*)(csb + 4096 + w * 512 + 256 + lane * 4) = cskv; }
  }
  f32x4 cs[2][4];
#pragma unroll
  for (int t = 0; t < 2; ++t)
#pragma unroll
    for (int g = 0; g < 4; ++g) cs[t][g] = *(const LAS f32x4*)(csb + 4096 + w * 512 + (t * 32 + 8 * g + 4 * hh) * 4);

  if (!ISQ) {
    char* act = p.ws + WS_ACT;
    const int ft = (cb - 5) * 2 + fh;
#pragma unroll
    for (int n = 0; n < 2; ++n) {
      const float m = st[n][0], r2 = st[n][1] * st[n][1];
      const int tile16 = (tok0 + 32 * n + l31) >> 4;
#pragma unroll
      for (int c = 0; c < 2; ++c) {
        float h[8];
#pragma unroll
        for (int j = 0; j < 8; ++j) {
          const int r = 8 * c + j;
          const float g = acc[0][n][r] - m * cs[0][r >> 2][r & 3], u = acc[1][n][r] - m * cs[1][r >> 2][r & 3];
          h[j] = fmaxf(g, 0.f) * u * r2;
        }
        u32x4 o = {pk_bf16(h[0], h[1]), pk_bf16(h[2], h[3]), pk_bf16(h[4], h[5]), pk_bf16(h[6], h[7])};
        *(u32x4*)(act + ((size_t)(tile16 * NSTEP + ft) * 64 + (2 * c + hh) * 16 + (lane & 15)) * 16) = o;
      }
    }
  } else {
    const unsigned qb = lds0 + G1_STG + 8192 + w * 9216;
#pragma unroll
    for (int n = 0; n < 2; ++n) {
      const float m = st[n][0], rs = st[n][1] * QSCALE;
#pragma unroll
      for (int t = 0; t < 2; ++t)
#pragma unroll
        for (int g = 0; g < 4; ++g) {
          float v[4];
#pragma unroll
          for (int j = 0; j < 4; ++j) v[j] = (acc[t][n][4 * g + j] - m * cs[t][g][j]) * rs;
          *(LAS u32x2*)(qb + (32 * (n ^ sw) + l31) * 144 + (32 * t + 8 * g + 4 * hh) * 2) = u32x2{pk_bf16(v[0], v[1]), pk_bf16(v[2], v[3])};
        }
    }
    char* Q = p.ws + WS_Q;
#pragma unroll
    for (int i = 0; i < 8; ++i) {
      const int row = 8 * i + (lane >> 3);
      u32x4 v = *(const LAS u32x4*)(qb + row * 144 + (lane & 7) * 16);
      *(u32x4*)(Q + (size_t)(tok0 + row) * 1280 + cb * 256 + fh * 128 + (lane & 7) * 16) = v;
    }
    const int ktile = (tok0 >> 5) + sw;
    char* kv = p.ws + WS_KV + (ktile >> 1) * 7168;
    if (MODE == 2) {
      const float m = st[0][0], rs = st[0][1];
      float k[12];
#pragma unroll
      for (int g = 0; g < 3; ++g) {
        const f32x4 c = *(const LAS f32x4*)(csb + 4096 + w * 512 + 256 + (8 * g + 4 * hh) * 4);
#pragma unroll
        for (int j = 0; j < 4; ++j) k[4 * g + j] = (acckv[4 * g + j] - m * c[j]) * rs;
      }
      u32x4 a = {pk_bf16(k[0], k[1]), pk_bf16(k[2], k[3]), pk_bf16(k[4], k[5]), pk_bf16(k[6], k[7])};
      u32x2 b = {pk_bf16(k[8], k[9]), pk_bf16(k[10], k[11])};
      float n2 = 0.f;
#pragma unroll
      for (int j = 0; j < 4; ++j) n2 += bf_lo(a[j]) * bf_lo(a[j]) + bf_hi(a[j]) * bf_hi(a[j]);
      if (hh == 0) n2 += bf_lo(b[0]) * bf_lo(b[0]) + bf_hi(b[0]) * bf_hi(b[0]) + bf_lo(b[1]) * bf_lo(b[1]) + bf_hi(b[1]) * bf_hi(b[1]);
      else b = u32x2{0x3f803f80u, 0x00003f80u};
      n2 = swap32_add(n2);
#pragma unroll
      for (int o = 16; o; o >>= 1) { const float y = __shfl_xor(n2, o); n2 = (y > n2 || y != y) ? y : n2; }
      const int slot = (l31 & 19) | ((l31 & 4) << 1) | ((l31 & 8) >> 1);
      *(u32x4*)(kv + (ktile & 1) * 1024 + (hh * 32 + slot) * 16) = a;
      *(u32x2*)(kv + 2048 + (hh * 32 + slot) * 16 + (ktile & 1) * 8) = b;
      if (lane == 0) ((float*)(p.ws + WS_KMAX))[ktile] = n2;
    }
    if (MODE == 3) {
      unsigned o[8];
#pragma unroll
      for (int g = 0; g < 4; ++g) {
        const f32x4 s0 = *(const f32x4*)(stats + ktile * 32 + 8 * g + 4 * hh), s1 = *(const f32x4*)(stats + ktile * 32 + 8 * g + 4 * hh + 2);
        o[2 * g] = pk_bf16((acckv[4 * g] - s0[0] * cskv) * s0[1], (acckv[4 * g + 1] - s0[2] * cskv) * s0[3]);
        o[2 * g + 1] = pk_bf16((acckv[4 * g + 2] - s1[0] * cskv) * s1[1], (acckv[4 * g + 3] - s1[2] * cskv) * s1[3]);
      }
      if (l31 >= 20) {
        const unsigned c = l31 == 20 ? 0x3f803f80u : 0u;
#pragma unroll
        for (int j = 0; j < 8; ++j) o[j] = c;
      }
      *(u32x4*)(kv + 3072 + (ktile & 1) * 2048 + lane * 16) = u32x4{o[0], o[1], o[2], o[3]};
      *(u32x4*)(kv + 3072 + (ktile & 1) * 2048 + 1024 + lane * 16) = u32x4{o[4], o[5], o[6], o[7]};
    }
  }
}

__global__ __launch_bounds__(512, 2) void k_gemm1(G1P p) {
  extern __shared__ __attribute__((aligned(16))) char smem[];
  const unsigned lds0 = (unsigned)(uintptr_t)(LAS char*)smem;
  const int b = blockIdx.x, xcd = b & 7, j = b >> 3;
  const int rb = (xcd & 1) * 4 + (j & 3), cb = (xcd >> 1) * 8 + (j >> 2);
  const int w = __builtin_amdgcn_readfirstlane(threadIdx.x >> 6), lane = threadIdx.x & 63;
  if (cb >= 5) gemm1_body<0>(p, lds0, rb, cb, w, lane);
  else if (cb == 4 || w >= 4) gemm1_body<1>(p, lds0, rb, cb, w, lane);
  else if (cb < 2) gemm1_body<2>(p, lds0, rb, cb, w, lane);
  else gemm1_body<3>(p, lds0, rb, cb, w, lane);
}

struct KVF { bf16x8 ka[2]; u32x4 kb; bf16x8 v[4]; };
DEV KVF kv_load(const char* kv, int pair, int lane) {
  KVF r; const char* b = kv + pair * 7168 + lane * 16;
  r.ka[0] = *(const bf16x8*)b; r.ka[1] = *(const bf16x8*)(b + 1024); r.kb = *(const u32x4*)(b + 2048);
#pragma unroll
  for (int i = 0; i < 4; ++i) r.v[i] = *(const bf16x8*)(b + 3072 + 1024 * i);
  return r;
}
DEV s16x4 kb_half(const u32x4& kb, int t) { return __builtin_bit_cast(s16x4, u32x2{kb[2 * t], kb[2 * t + 1]}); }
DEV f32x16 qk(const KVF& f, int t, bf16x8 qa, s16x4 qb) {
  f32x16 s = MFMA32(f.ka[t], qa, f32x16{});
  return __builtin_amdgcn_mfma_f32_32x32x8bf16_1k(kb_half(f.kb, t), qb, s, 0, 0, 0);
}
DEV f32x16 pv(bf16x8 v, f32x8 pp, f32x16 o) {
  return __builtin_amdgcn_smfmac_f32_32x32x32_bf16(v, __builtin_bit_cast(bf16x16, pp), o, (int)0xDDDDDDDDu, 0, 0);
}
constexpr float SAFE_BOUND = 48.f;

__global__ __launch_bounds__(512, 2) void k_attn(char* __restrict__ ws) {
  extern __shared__ __attribute__((aligned(16))) char smem[];
  const unsigned lds0 = (unsigned)(uintptr_t)(LAS char*)smem;
  const int w = __builtin_amdgcn_readfirstlane(threadIdx.x >> 6), lane = threadIdx.x & 63;
  const int kg = w & 3, rg = w >> 2, l31 = lane & 31, hh = lane >> 5;
  const int b = blockIdx.x, blk = (b & 7) * 32 + (b >> 3);
  const char* kv = ws + WS_KV + kg * 8 * 7168;
  KVF A = kv_load(kv, 0, lane);
  bf16x8 qa[4]; u32x2 qb[4]; float shift[4];
  float kmax = ((const float*)(ws + WS_KMAX))[kg * 16 + (lane & 15)];
#pragma unroll
  for (int r = 0; r < 4; ++r) {
    const char* qp = ws + WS_Q + (size_t)(blk * 8 + rg * 4 + r) * 1280 + l31 * 40 + hh * 8;
    const u32x2 a = *(const u32x2*)qp, c = *(const u32x2*)(qp + 16);
    qb[r] = *(const u32x2*)(qp + 32);
    qa[r] = __builtin_bit_cast(bf16x8, u32x4{a[0], a[1], c[0], c[1]});
  }
#pragma unroll
  for (int o = 8; o; o >>= 1) { const float y = __shfl_xor(kmax, o); kmax = (y > kmax || y != y) ? y : kmax; }
  bool unsafe = false;
#pragma unroll
  for (int r = 0; r < 4; ++r) {
    const u32x4 u = __builtin_bit_cast(u32x4, qa[r]);
    if (hh) qb[r] = u32x2{0u, 0u};
    float n2 = bf_lo(qb[r][0]) * bf_lo(qb[r][0]) + bf_hi(qb[r][0]) * bf_hi(qb[r][0]) + bf_lo(qb[r][1]) * bf_lo(qb[r][1]) + bf_hi(qb[r][1]) * bf_hi(qb[r][1]);
#pragma unroll
    for (int j = 0; j < 4; ++j) n2 += bf_lo(u[j]) * bf_lo(u[j]) + bf_hi(u[j]) * bf_hi(u[j]);
    n2 = swap32_add(n2);
    shift[r] = sqrtf(n2 * kmax) * 1.001f;
    unsafe |= !(shift[r] <= SAFE_BOUND);
  }
  if (__builtin_amdgcn_readfirstlane(__any(unsafe))) {
    float mx[4] = {-INFINITY, -INFINITY, -INFINITY, -INFINITY};
    for (int pr = 0; pr < 8; ++pr) {
      KVF f = kv_load(kv, pr, lane);
#pragma unroll
      for (int t = 0; t < 2; ++t)
#pragma unroll
        for (int r = 0; r < 4; ++r) {
          const f32x16 s = qk(f, t, qa[r], __builtin_bit_cast(s16x4, qb[r]));
#pragma unroll
          for (int i = 0; i < 16; ++i) mx[r] = fmaxf(mx[r], s[i]);
        }
    }
#pragma unroll
    for (int r = 0; r < 4; ++r) shift[r] = swap32_max(mx[r]);
  }
#pragma unroll
  for (int r = 0; r < 4; ++r) {
    const float s0 = (float)(__bf16)shift[r], s1 = (float)(__bf16)(shift[r] - s0), s2 = (float)(__bf16)(shift[r] - s0 - s1);
    shift[r] = s0 + s1 + s2;
    if (hh) qb[r] = u32x2{pk_bf16(-s0, -s1), pk_bf16(-s2, 0.f)};
  }
  f32x16 o[4] = {};
  f32x16 s = qk(A, 0, qa[0], __builtin_bit_cast(s16x4, qb[0]));
  KVF B;
  auto steps = [&](const KVF& cur, const KVF& nxt) {
#pragma unroll
    for (int i = 0; i < 8; ++i) {
      const int t = i >> 2, r = i & 3, rn = (i + 1) & 3;
      const f32x16 sn = i < 7 ? qk(cur, (i + 1) >> 2, qa[rn], __builtin_bit_cast(s16x4, qb[rn])) : qk(nxt, 0, qa[0], __builtin_bit_cast(s16x4, qb[0]));
      f32x8 p0, p1;
#pragma unroll
      for (int e = 0; e < 8; ++e) { p0[e] = __builtin_amdgcn_exp2f(s[e]); p1[e] = __builtin_amdgcn_exp2f(s[8 + e]); }
      o[r] = pv(cur.v[2 * t], p0, o[r]);
      o[r] = pv(cur.v[2 * t + 1], p1, o[r]);
      s = sn;
#pragma unroll
      for (int g = 0; g < 4; ++g) { __builtin_amdgcn_sched_group_barrier(0x8, 1, 0); __builtin_amdgcn_sched_group_barrier(0x400, 4, 0); }
    }
  };
  for (int pr = 0; pr < 8; pr += 2) {
    B = kv_load(kv, pr + 1, lane);
    steps(A, B);
    A = kv_load(kv, (pr + 2) & 7, lane);
    steps(B, A);
  }
  constexpr unsigned PART = 8 * 160 * 16, DEN = 4 * PART;
#pragma unroll
  for (int r = 0; r < 4; ++r) {
    const unsigned a = lds0 + kg * PART + ((rg * 4 + r) * 160 + l31 * 5 + hh) * 16;
    *(LAS f32x4*)a = f32x4{o[r][0], o[r][1], o[r][2], o[r][3]};
    *(LAS f32x4*)(a + 32) = f32x4{o[r][4], o[r][5], o[r][6], o[r][7]};
    if (hh == 0) *(LAS f32x4*)(a + 64) = f32x4{o[r][8], o[r][9], o[r][10], o[r][11]};
    else *(LAS f32x2*)(lds0 + DEN + ((kg * 8 + rg * 4 + r) * 32 + l31) * 8) = f32x2{o[r][8], shift[r]};
  }
  __syncthreads();
  char* act = ws + WS_ACT;
  for (int it = threadIdx.x; it < 640; it += 512) {
    const int tk = it & 7, ch = it >> 3, st = ch >> 2, kq = ch & 3;
    unsigned out[4];
#pragma unroll
    for (int u = 0; u < 2; ++u) {
      const int g = 8 * st + 4 * (kq >> 1) + (kq & 1) + 2 * u, head = g / 5;
      f32x2 dn[4]; float m = -INFINITY;
#pragma unroll
      for (int k = 0; k < 4; ++k) { dn[k] = *(const LAS f32x2*)(lds0 + DEN + ((k * 8 + tk) * 32 + head) * 8); m = fmaxf(m, dn[k][1]); }
      f32x4 a = {}; float den = 0.f;
#pragma unroll
      for (int k = 0; k < 4; ++k) {
        const float f = __builtin_amdgcn_exp2f(dn[k][1] - m);
        den += f * dn[k][0];
        a += f * *(const LAS f32x4*)(lds0 + k * PART + (tk * 160 + g) * 16);
      }
      const float inv = 1.f / den;
      out[2 * u] = pk_bf16(a[0] * inv, a[1] * inv); out[2 * u + 1] = pk_bf16(a[2] * inv, a[3] * inv);
    }
    *(u32x4*)(act + ((size_t)((blk >> 1) * NSTEP + NSTEP_H + st) * 64 + kq * 16 + (blk & 1) * 8 + tk) * 16) = u32x4{out[0], out[1], out[2], out[3]};
  }
}

struct G2P { const char* x; const char* wd; const char* wo; const char* ws; char* out; };
DEV void gload16(u32x4& d, const void* sbase, unsigned voff) { asm volatile("global_load_dwordx4 %0, %1, %2" : "=v"(d) : "v"(voff), "s"(sbase) : "memory"); }

__global__ __launch_bounds__(512, 2) void k_gemm2(G2P p) {
  extern __shared__ __attribute__((aligned(16))) char smem[];
  const unsigned lds0 = (unsigned)(uintptr_t)(LAS char*)smem;
  const int w = __builtin_amdgcn_readfirstlane(threadIdx.x >> 6), lane = threadIdx.x & 63;
  const int b = blockIdx.x, xcd = b & 7, j = b >> 3;
  const int rb = (xcd >> 1) * 8 + (j & 7), cb = (xcd & 1) * 4 + (j >> 3);
  unsigned woff[5];
#pragma unroll
  for (int u = 0; u < 5; ++u) { const int id = 64 * u + lane, row = id / 10; woff[u] = row * 1280 + (id - row * 10) * 16 + cb * 160; }
  const char* act = p.ws + WS_ACT + (size_t)rb * 4 * NSTEP * 1024;
  const unsigned ring = lds0 + w * 15360;
  u32x4 bq[3][4];
  auto issue = [&](int i, int slot) {
    int st = w + 8 * i; if (st >= NSTEP) st = NSTEP - 1;
    const char* wsrc = st < NSTEP_H ? p.wd + st * 32 * 1280 : p.wo + (st - NSTEP_H) * 32 * 1280;
#pragma unroll
    for (int u = 0; u < 5; ++u) glds16(wsrc, woff[u], ring + slot * 5120 + u * 1024);
#pragma unroll
    for (int n = 0; n < 4; ++n) gload16(bq[slot][n], act + (n * NSTEP + st) * 1024, lane * 16);
  };
  const int kq = lane >> 4;
  const unsigned ta = ring + (16 * (kq >> 1) + 4 * (kq & 1) + ((lane & 15) >> 2)) * 160 + (lane & 3) * 8;
  f32x4 acc[5][4] = {};
  issue(0, 0); issue(1, 1);
#pragma unroll
  for (int i = 0; i < 10; ++i) {
    const int slot = i % 3;
    if (i < 8) { issue(i + 2, (i + 2) % 3);
      asm volatile("s_waitcnt vmcnt(18)" : "+v"(bq[slot][0]), "+v"(bq[slot][1]), "+v"(bq[slot][2]), "+v"(bq[slot][3]) :: "memory"); }
    else if (i == 8) asm volatile("s_waitcnt vmcnt(9)" : "+v"(bq[slot][0]), "+v"(bq[slot][1]), "+v"(bq[slot][2]), "+v"(bq[slot][3]) :: "memory");
    else asm volatile("s_waitcnt vmcnt(0)" : "+v"(bq[slot][0]), "+v"(bq[slot][1]), "+v"(bq[slot][2]), "+v"(bq[slot][3]) :: "memory");
    if (i < 9 || w + 72 < NSTEP) {
#pragma unroll
      for (int m = 0; m < 5; ++m) {
        const bf16x8 a = cat8(lds_tr(ta + slot * 5120 + m * 32), lds_tr(ta + slot * 5120 + m * 32 + 8 * 160));
#pragma unroll
        for (int n = 0; n < 4; ++n) acc[m][n] = MFMA16(a, __builtin_bit_cast(bf16x8, bq[slot][n]), acc[m][n]);
      }
    }
    asm volatile("s_waitcnt lgkmcnt(0)" ::: "memory");
  }
  __syncthreads();
#pragma unroll
  for (int m = 0; m < 5; ++m)
#pragma unroll
    for (int n = 0; n < 4; ++n) *(LAS f32x4*)(lds0 + w * 20480 + ((m * 4 + n) * 64 + lane) * 16) = acc[m][n];
  __syncthreads();
  for (int it = threadIdx.x; it < 640; it += 512) {
    const int tok = it / 10, ch = it - tok * 10;
    const size_t go = (size_t)(rb * 64 + tok) * 1280 + cb * 160 + ch * 16;
    const u32x4 xv = *(const u32x4*)(p.x + go);
    const unsigned a = lds0 + (((ch >> 1) * 4 + (tok >> 4)) * 64 + (ch & 1) * 32 + (tok & 15)) * 16;
    f32x4 s0 = {}, s1 = {};
#pragma unroll
    for (int k = 0; k < 8; ++k) { s0 += *(const LAS f32x4*)(a + k * 20480); s1 += *(const LAS f32x4*)(a + k * 20480 + 256); }
    *(u32x4*)(p.out + go) = u32x4{pk_bf16(bf_lo(xv[0]) + s0[0], bf_hi(xv[0]) + s0[1]), pk_bf16(bf_lo(xv[1]) + s0[2], bf_hi(xv[1]) + s0[3]),
                                  pk_bf16(bf_lo(xv[2]) + s1[0], bf_hi(xv[2]) + s1[1]), pk_bf16(bf_lo(xv[3]) + s1[2], bf_hi(xv[3]) + s1[3])};
  }
}

extern "C" void kernel_launch(void* const* d_in, const int* in_sizes, int n_in, void* d_out, int out_size, void* d_ws, size_t ws_size,
                              hipStream_t stream) {
  const char* x = (const char*)d_in[0];
  char* ws = (char*)d_ws;
  (void)hipFuncSetAttribute((const void*)k_gemm1, hipFuncAttributeMaxDynamicSharedMemorySize, 163840);
  (void)hipFuncSetAttribute((const void*)k_attn, hipFuncAttributeMaxDynamicSharedMemorySize, 163840);
  (void)hipFuncSetAttribute((const void*)k_gemm2, hipFuncAttributeMaxDynamicSharedMemorySize, 163840);
  k_stats<<<256, 512, 0, stream>>>((const unsigned*)x, (f32x2*)(ws + WS_STATS));
  G1P p1{x, (const char*)d_in[1], (const char*)d_in[2], (const char*)d_in[3], (const char*)d_in[6], (const char*)d_in[7], ws};
  k_gemm1<<<256, 512, 3 * G1_STG, stream>>>(p1);
  k_attn<<<256, 512, 4 * 8 * 160 * 16 + 8192, stream>>>(ws);
  G2P p2{x, (const char*)d_in[8], (const char*)d_in[4], ws, (char*)d_out};
  k_gemm2<<<256, 512, 163840, stream>>>(p2);
}
```

```cpp
#include <hip/hip_runtime.h>
#include <stdint.h>

#define KERNEL_ASM \
  "s_load_dwordx16 s[8:23], %[ka], 0x0\n\t" \
  "s_load_dwordx4 s[24:27], %[ka], 0x40\n\t" \
  "s_mov_b32 s35, %[bid]\n\t" \
  "v_readfirstlane_b32 s32, v0\n\t" \
  "v_and_b32 v228, 63, v0\n\t" \
  "s_and_b32 s60, s35, 7\n\t" \
  "v_lshlrev_b32 v237, 4, v228\n\t" \
  "s_lshr_b32 s61, s35, 3\n\t" \
  "v_lshrrev_b32 v1, 3, v228\n\t" \
  "s_lshr_b32 s32, s32, 6\n\t" \
  "v_mul_u32_u24 v1, 0x500, v1\n\t" \
  "s_and_b32 s63, s60, 1\n\t" \
  "v_xor_b32 v2, v237, v228\n\t" \
  "s_and_b32 s57, s61, 3\n\t" \
  "v_and_b32 v2, 0x70, v2\n\t" \
  "s_lshl2_add_u32 s57, s63, s57\n\t" \
  "v_or_b32 v170, v1, v2\n\t" \
  "s_lshr_b32 s63, s60, 1\n\t" \
  "v_xor_b32 v171, 64, v170\n\t" \
  "s_lshr_b32 s64, s61, 2\n\t" \
  "v_add_u32 v171, 10240, v171\n\t" \
  "s_lshl_b32 s65, s32, 1\n\t" \
  "v_and_b32 v3, 3, v228\n\t" \
  "s_and_b32 s65, s65, 2\n\t" \
  "v_bfe_u32 v4, v228, 2, 3\n\t" \
  "s_and_b32 s34, s32, 3\n\t" \
  "v_xor_b32 v3, s65, v3\n\t" \
  "s_lshr_b32 s68, s32, 2\n\t" \
  "v_lshlrev_b32 v3, 4, v3\n\t" \
  "s_lshl_b32 s66, s57, 8\n\t" \
  "v_lshl_or_b32 v4, s32, 3, v4\n\t" \
  "s_mov_b32 s56, 0\n\t" \
  "v_lshlrev_b32 v5, 1, v228\n\t" \
  "v_and_or_b32 v5, v5, 64, v3\n\t" \
  "v_lshlrev_b32 v192, 2, v228\n\t" \
  "v_mov_b32 v9, 0\n\t" \
  "s_getpc_b64 s[28:29]\n\t" \
  ".Lpc%=:\n\t" \
  "s_lshl3_add_u32 s67, s61, s32\n\t" \
  "v_lshlrev_b32 v60, 8, v228\n\t" \
  "v_add_lshl_u32 v60, v60, s67, 7\n\t" \
  "v_min_u32 v60, .Lend%=-.Lpc%=, v60\n\t" \
  "s_mov_b64 exec, 3\n\t" \
  "global_load_dword v195, v60, s[28:29]\n\t" \
  "s_mov_b64 exec, -1\n\t" \
  "s_cmp_lt_u32 s35, 64\n\t" \
  "s_cbranch_scc0 .Lf%=\n\t" \
  "s_lshl1_add_u32 s33, s63, s64\n\t" \
  "v_min_u32 v3, 24, v3\n\t" \
  "v_mad_u32_u24 v3, v4, 40, v3\n\t" \
  "v_mul_u32_u24 v6, 0x500, v4\n\t" \
  "v_add_u32 v172, v6, v5\n\t" \
  "s_cmp_eq_u32 s63, 3\n\t" \
  "s_cselect_b64 vcc, -1, 0\n\t" \
  "s_movk_i32 s70, 1280\n\t" \
  "s_cmov_b32 s70, 40\n\t" \
  "v_cndmask_b32 v173, v172, v3, vcc\n\t" \
  "s_lshl_b32 s52, s70, 6\n\t" \
  "s_lshl_b32 s48, s32, 11\n\t" \
  "s_mul_i32 s50, s32, 3072\n\t" \
  "s_add_u32 s50, s50, 16384\n\t" \
  "s_lshl3_add_u32 s69, s33, s32\n\t" \
  "s_add_u32 s69, s69, s66\n\t" \
  "v_lshrrev_b32 v6, 2, v228\n\t" \
  "v_and_b32 v6, 2, v6\n\t" \
  "v_lshrrev_b32 v7, 3, v228\n\t" \
  "v_and_or_b32 v6, v7, 4, v6\n\t" \
  "v_lshlrev_b32 v7, 1, v228\n\t" \
  "v_and_or_b32 v6, v7, 32, v6\n\t" \
  "v_mul_u32_u24 v6, 0x500, v6\n\t" \
  "v_or_b32 v170, v6, v2\n\t" \
  "v_xor_b32 v171, 64, v170\n\t" \
  "v_add_u32 v171, 10240, v171\n\t" \
  "s_lshl1_add_u32 s64, s64, s68\n\t" \
  "s_lshl_b32 s64, s64, 6\n\t" \
  "s_add_u32 s66, s66, s64\n\t" \
  "s_mul_i32 s64, s34, 9\n\t" \
  "s_and_b32 s64, s64, 17\n\t" \
  "s_add_u32 s66, s66, s64\n\t" \
  "s_mul_i32 s63, s63, 384\n\t" \
  "s_mul_i32 s66, s66, 1280\n\t" \
  "s_mul_i32 s55, s69, 1280\n\t" \
  "s_waitcnt lgkmcnt(0)\n\t" \
  "s_add_u32 s46, s8, s55\n\t" \
  "s_addc_u32 s47, s9, 0\n\t" \
  "s_add_u32 s36, s8, s66\n\t" \
  "s_addc_u32 s37, s9, 0\n\t" \
  "global_load_dwordx4 v[10:13], v237, s[46:47]\n\t" \
  "global_load_dword v14, v192, s[46:47] offset:1024\n\t" \
  "global_load_dwordx2 v[16:17], v9, s[24:25] sc1\n\t" \
  "s_add_u32 s40, s10, s63\n\t" \
  "s_addc_u32 s41, s11, 0\n\t" \
  "s_add_u32 s42, s40, 128\n\t" \
  "s_addc_u32 s43, s41, 0\n\t" \
  "s_add_u32 s44, s40, 256\n\t" \
  "s_addc_u32 s45, s41, 0\n\t" \
  "s_cmp_ge_u32 s33, 6\n\t" \
  "s_cmov_b64 s[42:43], s[12:13]\n\t" \
  "s_cmov_b64 s[44:45], s[14:15]\n\t" \
  "s_add_u32 m0, s50, s56\n\t" \
  "s_nop 0\n\t" \
  "global_load_lds_dwordx4 v172, s[40:41]\n\t" \
  "s_add_u32 m0, m0, 0x400\n\t" \
  "v_add_u32 v172, 81920, v172\n\t" \
  "global_load_lds_dwordx4 v173, s[42:43]\n\t" \
  "s_add_u32 m0, m0, 0x400\n\t" \
  "s_nop 0\n\t" \
  "global_load_lds_dwordx4 v173, s[44:45]\n\t" \
  "v_add_u32 v173, s52, v173\n\t" \
  "s_branch .Lq2%=\n\t" \
  ".Lf%=:\n\t" \
  "s_mul_i32 s58, s63, 6\n\t" \
  "s_add_u32 s58, s58, s64\n\t" \
  "s_add_u32 s33, s58, 6\n\t" \
  "s_sub_u32 s58, s58, 2\n\t" \
  "v_mul_u32_u24 v4, 0xd80, v4\n\t" \
  "v_add_u32 v172, v4, v5\n\t" \
  "s_lshl_b32 s59, s33, 1\n\t" \
  "s_and_b32 s59, s59, 2\n\t" \
  "v_bfe_u32 v6, v228, 1, 2\n\t" \
  "s_lshl_b32 s48, s32, 12\n\t" \
  "v_and_or_b32 v6, v228, 8, v6\n\t" \
  "s_add_u32 s49, s48, 2048\n\t" \
  "v_lshrrev_b32 v7, 2, v228\n\t" \
  "s_lshl_b32 s50, s32, 11\n\t" \
  "v_and_or_b32 v6, v7, 4, v6\n\t" \
  "s_add_u32 s50, s50, 32768\n\t" \
  "v_lshl_or_b32 v6, s34, 4, v6\n\t" \
  "s_lshl_b32 s51, s32, 9\n\t" \
  "v_mul_u32_u24 v6, 0xd80, v6\n\t" \
  "s_add_u32 s51, s51, 49152\n\t" \
  "v_lshlrev_b32 v7, 5, v228\n\t" \
  "s_lshl3_add_u32 s69, s33, s32\n\t" \
  "v_and_or_b32 v173, v7, 32, v6\n\t" \
  "s_add_u32 s69, s69, s66\n\t" \
  "s_xor_b32 s65, s65, s59\n\t" \
  "s_lshl4_add_u32 s66, s65, s66\n\t" \
  "s_lshr_b32 s65, s32, 1\n\t" \
  "s_lshl_b32 s65, s65, 6\n\t" \
  "s_add_u32 s66, s66, s65\n\t" \
  "s_lshl_b32 s63, s58, 7\n\t" \
  "s_lshr_b32 s67, s58, 2\n\t" \
  "s_lshl_b32 s67, s67, 6\n\t" \
  "s_and_b32 s65, s58, 2\n\t" \
  "s_lshl3_add_u32 s67, s65, s67\n\t" \
  "s_add_u32 s67, s67, 3072\n\t" \
  "s_mul_i32 s66, s66, 1280\n\t" \
  "s_mul_i32 s55, s69, 1280\n\t" \
  "s_waitcnt lgkmcnt(0)\n\t" \
  "s_add_u32 s46, s8, s55\n\t" \
  "s_addc_u32 s47, s9, 0\n\t" \
  "s_add_u32 s36, s8, s66\n\t" \
  "s_addc_u32 s37, s9, 0\n\t" \
  "global_load_dwordx4 v[10:13], v237, s[46:47]\n\t" \
  "global_load_dword v14, v192, s[46:47] offset:1024\n\t" \
  "global_load_dwordx2 v[16:17], v9, s[24:25] sc1\n\t" \
  "s_add_u32 s40, s18, s63\n\t" \
  "s_addc_u32 s41, s19, 0\n\t" \
  "s_add_u32 s42, s20, s63\n\t" \
  "s_addc_u32 s43, s21, 0\n\t" \
  "s_add_u32 s38, s36, 20480\n\t" \
  "s_addc_u32 s39, s37, 0\n\t" \
  "s_add_u32 s18, s18, s67\n\t" \
  "s_addc_u32 s19, s19, 0\n\t" \
  "s_add_u32 s20, s20, s67\n\t" \
  "s_addc_u32 s21, s21, 0\n\t" \
  "s_cmp_lt_u32 s32, 4\n\t" \
  "s_cselect_b64 s[44:45], s[18:19], s[20:21]\n\t" \
  "v_mov_b64 v[18:19], s[40:41]\n\t" \
  "v_bfe_u32 v6, v228, 2, 4\n\t" \
  "v_mov_b32 v21, 0\n\t" \
  "v_mov_b32 v7, 0xd80\n\t" \
  "s_mov_b32 exec_lo, 0x88888888\n\t" \
  "s_mov_b32 exec_hi, 0x88888888\n\t" \
  "v_mov_b64 v[18:19], s[20:21]\n\t" \
  "s_mov_b32 exec_lo, 0x44444444\n\t" \
  "s_mov_b32 exec_hi, 0x44444444\n\t" \
  "v_mov_b64 v[18:19], s[18:19]\n\t" \
  "s_and_b32 s63, s58, 7\n\t" \
  "s_cmp_eq_u32 s63, s32\n\t" \
  "s_cselect_b32 s0, -1, 0x33333333\n\t" \
  "s_mov_b32 exec_lo, 0x22222222\n\t" \
  "s_mov_b32 exec_hi, 0x22222222\n\t" \
  "v_mov_b64 v[18:19], s[42:43]\n\t" \
  "s_mov_b64 exec, -1\n\t" \
  "s_lshl3_add_u32 s1, s57, s32\n\t" \
  "s_mul_i32 s63, s1, 3\n\t" \
  "s_add_u32 s63, s63, 0\n\t" \
  "v_add_u32 v20, s63, v6\n\t" \
  "v_mul_u32_u24 v20, v20, v7\n\t" \
  "v_lshl_add_u64 v[22:23], v[18:19], 0, v[20:21]\n\t" \
  "s_and_b32 exec_lo, s0, 4095\n\t" \
  "s_and_b32 exec_hi, s0, 0\n\t" \
  "global_load_dword v194, v[22:23], off\n\t" \
  "s_mov_b64 exec, -1\n\t" \
  "s_lshl3_add_u32 s63, s35, s32\n\t" \
  "s_sub_u32 s63, s63, 512\n\t" \
  "s_cmp_ge_u32 s63, 1280\n\t" \
  "s_cmov_b64 s[10:11], s[12:13]\n\t" \
  "s_cselect_b32 s64, 1280, 0\n\t" \
  "s_cmp_ge_u32 s63, 1320\n\t" \
  "s_cmov_b64 s[10:11], s[14:15]\n\t" \
  "s_cmov_b32 s64, 1320\n\t" \
  "s_min_u32 s63, s63, 1359\n\t" \
  "s_sub_u32 s63, s63, s64\n\t" \
  "s_mul_i32 s63, s63, 5\n\t" \
  "v_add_lshl_u32 v20, v228, s63, 7\n\t" \
  "s_mov_b64 exec, 31\n\t" \
  "global_load_dword v196, v20, s[10:11]\n\t" \
  "s_mov_b64 exec, -1\n\t" \
  "s_add_u32 m0, s50, s56\n\t" \
  "s_nop 0\n\t" \
  "global_load_lds_dwordx4 v172, s[40:41]\n\t" \
  "s_add_u32 m0, m0, 0x400\n\t" \
  "s_nop 0\n\t" \
  "global_load_lds_dwordx4 v172, s[42:43]\n\t" \
  "s_add_u32 m0, s48, s56\n\t" \
  "v_add_u32 v172, 221184, v172\n\t" \
  "global_load_lds_dwordx4 v170, s[36:37]\n\t" \
  "s_add_u32 m0, m0, 0x400\n\t" \
  "s_nop 0\n\t" \
  "global_load_lds_dwordx4 v171, s[36:37]\n\t" \
  "s_and_b32 s63, s61, 3\n\t" \
  "s_lshl_b32 s52, 2, s63\n\t" \
  "s_or_b32 s52, s52, 0x80\n\t" \
  "s_lshl3_add_u32 s63, s58, s32\n\t" \
  "s_cmp_ge_u32 s63, 183\n\t" \
  "s_cmovk_i32 s52, 0x80\n\t" \
  "s_mul_i32 s63, s63, 1792\n\t" \
  "s_min_u32 s63, s63, 325888\n\t" \
  "s_mul_i32 s64, s57, 327680\n\t" \
  "s_add_u32 s63, s63, s64\n\t" \
  "s_add_u32 s63, s63, 10522624\n\t" \
  "s_add_u32 s6, s24, s63\n\t" \
  "s_addc_u32 s7, s25, 0\n\t" \
  "v_mov_b64 v[188:189], -1\n\t" \
  "v_mov_b64 v[190:191], -1\n\t" \
  "v_and_b32 v24, 15, v228\n\t" \
  "v_lshrrev_b32 v25, 4, v228\n\t" \
  "v_and_b32 v229, 48, v228\n\t" \
  "v_bfe_u32 v26, v228, 2, 2\n\t" \
  "v_and_b32 v28, 3, v228\n\t" \
  "v_lshlrev_b32 v27, 6, v26\n\t" \
  "v_lshl_or_b32 v27, v28, 3, v27\n\t" \
  "v_lshlrev_b32 v29, 1, v229\n\t" \
  "v_and_b32 v29, 32, v29\n\t" \
  "v_or_b32 v27, v27, v29\n\t" \
  "v_lshrrev_b32 v30, 1, v24\n\t" \
  "v_xor_b32 v30, v30, v25\n\t" \
  "v_lshlrev_b32 v30, 4, v30\n\t" \
  "v_lshl_or_b32 v30, v24, 7, v30\n\t" \
  "v_cmp_gt_u32 vcc, 2, v24\n\t" \
  "v_mov_b32 v33, 0x3f803f80\n\t" \
  "v_mov_b32 v174, 0xeeeeeeee\n\t" \
  "v_cndmask_b32 v176, 0, v33, vcc\n\t" \
  "v_mov_b32 v34, 0x44444444\n\t" \
  "v_cmp_eq_u32 vcc, 0, v24\n\t" \
  "v_mov_b32 v177, v176\n\t" \
  "v_mov_b64 v[178:179], v[176:177]\n\t" \
  "v_cndmask_b32 v174, v174, v34, vcc\n\t" \
  "s_add_u32 s64, s68, s59\n\t" \
  "s_lshl2_add_u32 s63, s34, s68\n\t" \
  "v_lshl_add_u32 v164, s63, 11, v30\n\t" \
  "v_xor_b32 v165, 0x800, v164\n\t" \
  "v_xor_b32 v166, 64, v164\n\t" \
  "v_xor_b32 v167, 64, v165\n\t" \
  "s_lshl_b32 s64, s64, 8\n\t" \
  "v_lshl_or_b32 v180, v24, 4, s64\n\t" \
  "v_lshl_or_b32 v27, v25, 11, v27\n\t" \
  "v_lshlrev_b32 v31, 5, v26\n\t" \
  "v_lshl_or_b32 v31, v28, 3, v31\n\t" \
  "v_lshl_or_b32 v31, v29, 2, v31\n\t" \
  "v_and_b32 v32, 32, v228\n\t" \
  "v_lshl_or_b32 v31, v32, 4, v31\n\t" \
  "s_lshl_b32 s63, s68, 11\n\t" \
  "s_add_u32 s63, s63, 49152\n\t" \
  "v_add_u32 v168, s63, v31\n\t" \
  "v_xor_b32 v169, 0x800, v168\n\t" \
  "s_and_b32 s63, s34, 2\n\t" \
  "s_add_u32 s63, s63, s68\n\t" \
  "s_lshl_b32 s63, s63, 4\n\t" \
  "s_and_b32 s64, s34, 1\n\t" \
  "s_or_b32 s63, s63, s64\n\t" \
  "s_lshl_b32 s63, s63, 5\n\t" \
  "s_add_u32 s63, s63, 32768\n\t" \
  "v_xor_b32 v160, s63, v27\n\t" \
  "v_xor_b32 v161, 32, v160\n\t" \
  "v_xor_b32 v162, 0x400, v160\n\t" \
  "v_xor_b32 v163, 0x420, v160\n\t" \
  "s_lshl2_add_u32 s64, s57, s34\n\t" \
  "v_xor_b32 v181, 256, v180\n\t" \
  "v_xor_b32 v182, 512, v180\n\t" \
  "v_xor_b32 v183, 768, v180\n\t" \
  "s_mov_b32 s71, s64\n\t" \
  "s_lshl_b32 s64, s64, 10\n\t" \
  "s_add_u32 s64, s64, 4096\n\t" \
  "s_add_u32 s46, s24, s64\n\t" \
  "s_addc_u32 s47, s25, 0\n\t" \
  "s_lshl_b32 s63, s60, 15\n\t" \
  "s_add_u32 s63, s63, 65536\n\t" \
  "s_add_u32 s30, s24, s63\n\t" \
  "s_addc_u32 s31, s25, 0\n\t" \
  "s_lshl_b32 s62, s60, 5\n\t" \
  "s_lshr_b32 s63, s61, 4\n\t" \
  "s_or_b32 s63, s63, s62\n\t" \
  "v_and_or_b32 v233, v228, 6, s63\n\t" \
  "s_lshl_b32 s63, s32, 6\n\t" \
  "s_add_u32 s63, s63, 6\n\t" \
  "v_and_or_b32 v31, v237, 32, s63\n\t" \
  "s_mov_b32 exec_lo, 0\n\t" \
  "v_and_or_b32 v233, v228, 1, v31\n\t" \
  "s_mov_b32 exec_lo, -1\n\t" \
  "v_lshlrev_b32 v233, 7, v233\n\t" \
  "s_add_u32 s62, s62, s61\n\t" \
  "s_lshl_b32 s63, s57, 5\n\t" \
  "s_add_u32 s63, s63, s33\n\t" \
  "s_lshl_b32 s63, s63, 7\n\t" \
  "s_add_u32 s63, s63, 65536\n\t" \
  "v_lshrrev_b32 v31, 3, v228\n\t" \
  "v_lshl_add_u32 v31, v31, 15, s63\n\t" \
  "v_and_b32 v32, 0x70, v237\n\t" \
  "v_add_u32 v231, v31, v32\n\t" \
  "s_add_u32 m0, s49, s56\n\t" \
  "s_nop 0\n\t" \
  "global_load_lds_dwordx4 v170, s[38:39]\n\t" \
  "s_add_u32 m0, m0, 0x400\n\t" \
  "v_add_u32 v170, 0x80, v170\n\t" \
  "global_load_lds_dwordx4 v171, s[38:39]\n\t" \
  "v_add_u32 v171, 0x80, v171\n\t" \
  "s_add_u32 m0, s51, s56\n\t" \
  "s_mov_b32 exec_hi, 0\n\t" \
  "global_load_lds_dwordx4 v173, s[44:45]\n\t" \
  "s_mov_b32 exec_hi, -1\n\t" \
  "v_add_u32 v173, 221184, v173\n\t" \
  "s_barrier\n\t" \
  "s_mov_b32 s56, 53248\n\t" \
  "s_add_u32 m0, s50, s56\n\t" \
  "s_nop 0\n\t" \
  "global_load_lds_dwordx4 v172, s[40:41]\n\t" \
  "s_add_u32 m0, m0, 0x400\n\t" \
  "s_nop 0\n\t" \
  "global_load_lds_dwordx4 v172, s[42:43]\n\t" \
  "s_add_u32 m0, s48, s56\n\t" \
  "v_add_u32 v172, 221184, v172\n\t" \
  "global_load_lds_dwordx4 v170, s[36:37]\n\t" \
  "s_add_u32 m0, m0, 0x400\n\t" \
  "s_nop 0\n\t" \
  "global_load_lds_dwordx4 v171, s[36:37]\n\t" \
  "s_waitcnt vmcnt(11)\n\t" \
  "v_dot2_f32_bf16 v35, v10, v33, 0\n\t" \
  "v_dot2_f32_bf16 v36, v10, v10, 0\n\t" \
  "v_dot2_f32_bf16 v35, v11, v33, v35\n\t" \
  "v_dot2_f32_bf16 v36, v11, v11, v36\n\t" \
  "v_dot2_f32_bf16 v35, v12, v33, v35\n\t" \
  "v_dot2_f32_bf16 v36, v12, v12, v36\n\t" \
  "v_dot2_f32_bf16 v35, v13, v33, v35\n\t" \
  "v_dot2_f32_bf16 v36, v13, v13, v36\n\t" \
  "v_dot2_f32_bf16 v35, v14, v33, v35\n\t" \
  "v_dot2_f32_bf16 v36, v14, v14, v36\n\t" \
  "v_readfirstlane_b32 s66, v16\n\t" \
  "v_readfirstlane_b32 s67, v17\n\t" \
  "s_lshl_b32 s63, s69, 4\n\t" \
  "v_add_f32_dpp v35, v35, v35 quad_perm:[1,0,3,2] row_mask:0xf bank_mask:0xf bound_ctrl:1\n\t" \
  "v_add_f32_dpp v36, v36, v36 quad_perm:[1,0,3,2] row_mask:0xf bank_mask:0xf bound_ctrl:1\n\t" \
  "s_add_u32 s63, s63, 4096\n\t" \
  "v_add_f32_dpp v35, v35, v35 quad_perm:[2,3,0,1] row_mask:0xf bank_mask:0xf bound_ctrl:1\n\t" \
  "v_add_f32_dpp v36, v36, v36 quad_perm:[2,3,0,1] row_mask:0xf bank_mask:0xf bound_ctrl:1\n\t" \
  "v_mov_b32 v9, s63\n\t" \
  "v_add_f32_dpp v35, v35, v35 row_half_mirror row_mask:0xf bank_mask:0xf bound_ctrl:1\n\t" \
  "v_add_f32_dpp v36, v36, v36 row_half_mirror row_mask:0xf bank_mask:0xf bound_ctrl:1\n\t" \
  "s_mul_i32 s63, s67, 0x4c957f2d\n\t" \
  "v_add_f32_dpp v35, v35, v35 row_mirror row_mask:0xf bank_mask:0xf bound_ctrl:1\n\t" \
  "v_add_f32_dpp v36, v36, v36 row_mirror row_mask:0xf bank_mask:0xf bound_ctrl:1\n\t" \
  "s_mul_i32 s64, s66, 0x5851f42d\n\t" \
  "v_mov_b32 v46, v35\n\t" \
  "v_mov_b32 v47, v36\n\t" \
  "s_add_u32 s63, s63, s64\n\t" \
  "v_permlane16_swap_b32 v35, v46\n\t" \
  "v_permlane16_swap_b32 v36, v47\n\t" \
  "s_mul_hi_u32 s64, s66, 0x4c957f2d\n\t" \
  "v_add_f32 v35, v35, v46\n\t" \
  "v_add_f32 v36, v36, v47\n\t" \
  "s_add_u32 s63, s63, s64\n\t" \
  "v_mov_b32 v46, v35\n\t" \
  "v_mov_b32 v47, v36\n\t" \
  "s_mul_i32 s64, s66, 0x4c957f2d\n\t" \
  "v_permlane32_swap_b32 v35, v46\n\t" \
  "v_permlane32_swap_b32 v36, v47\n\t" \
  "s_add_u32 s28, s64, 0xf767814f\n\t" \
  "v_add_f32 v35, v35, v46\n\t" \
  "v_add_f32 v36, v36, v47\n\t" \
  "s_addc_u32 s29, s63, 0x14057b7e\n\t" \
  "v_mul_f32 v48, 0x3acccccd, v35\n\t" \
  "v_mul_f32 v36, 0x3acccccd, v36\n\t" \
  "v_fma_f32 v50, -v48, v48, v36\n\t" \
  "v_mul_f32 v36, 0x3b800000, v36\n\t" \
  "v_cmp_gt_f32 vcc, v36, v50\n\t" \
  "v_mov_b32 v49, s28\n\t" \
  "v_mov_b32 v51, s28\n\t" \
  "s_cbranch_vccnz .Lvarf%=\n\t" \
  ".Lvardf%=:\n\t" \
  "v_add_f32 v50, 0x3727c5ac, v50\n\t" \
  "v_mov_b64 v[80:81], 0\n\t" \
  "v_rsq_f32 v50, v50\n\t" \
  "s_mov_b32 s53, 0\n\t" \
  "s_mov_b32 s54, 0\n\t" \
  "s_mov_b32 s55, 53248\n\t" \
  "s_mov_b32 s66, -106496\n\t" \
  "s_mov_b32 s67, 53248\n\t" \
  "s_add_u32 m0, s49, s56\n\t" \
  "s_nop 0\n\t" \
  "global_load_lds_dwordx4 v170, s[38:39]\n\t" \
  "s_add_u32 m0, m0, 0x400\n\t" \
  "v_add_u32 v170, 0x80, v170\n\t" \
  "global_load_lds_dwordx4 v171, s[38:39]\n\t" \
  "v_add_u32 v171, 0x80, v171\n\t" \
  "s_add_u32 m0, s51, s56\n\t" \
  "s_mov_b32 exec_hi, 0\n\t" \
  "global_load_lds_dwordx4 v173, s[44:45]\n\t" \
  "s_mov_b32 exec_hi, -1\n\t" \
  "v_add_u32 v173, 221184, v173\n\t" \
  "s_mul_i32 s63, s1, 7\n\t" \
  "s_add_u32 s63, s63, 192\n\t" \
  "v_add_u32 v20, s63, v6\n\t" \
  "v_mul_u32_u24 v20, v20, v7\n\t" \
  "v_lshl_add_u64 v[22:23], v[18:19], 0, v[20:21]\n\t" \
  "s_and_b32 exec_lo, s0, 268435455\n\t" \
  "s_and_b32 exec_hi, s0, 0\n\t" \
  "global_load_dword v185, v[22:23], off\n\t" \
  "s_mov_b64 exec, -1\n\t" \
  "s_mov_b32 s56, 106496\n\t" \
  "s_mov_b64 exec, 1\n\t" \
  "global_store_dwordx4 v9, v[48:51], s[24:25] sc1\n\t" \
  "s_mov_b64 exec, -1\n\t" \
  "v_mfma_f32_32x32x8_bf16 v[64:79], v[80:81], v[80:81], 0\n\t" \
  "v_mfma_f32_32x32x8_bf16 v[32:47], v[80:81], v[80:81], 0\n\t" \
  "v_mfma_f32_32x32x8_bf16 v[16:31], v[80:81], v[80:81], 0\n\t" \
  "v_mfma_f32_32x32x8_bf16 v[0:15], v[80:81], v[80:81], 0\n\t" \
  "v_mfma_f32_32x32x8_bf16 v[48:63], v[80:81], v[80:81], 0\n\t" \
  "s_waitcnt vmcnt(9)\n\t" \
  "s_barrier\n\t" \
  "ds_read_b128 v[112:115], v164 offset:0\n\t" \
  "ds_read_b64_tr_b16 v[80:81], v160 offset:0\n\t" \
  "ds_read_b64_tr_b16 v[82:83], v160 offset:256\n\t" \
  "ds_read_b128 v[116:119], v165 offset:0\n\t" \
  "ds_read_b128 v[120:123], v164 offset:4096\n\t" \
  "ds_read_b128 v[124:127], v165 offset:4096\n\t" \
  "ds_read_b64_tr_b16 v[88:89], v161 offset:0\n\t" \
  "ds_read_b64_tr_b16 v[90:91], v161 offset:256\n\t" \
  "ds_read_b64_tr_b16 v[96:97], v162 offset:0\n\t" \
  "ds_read_b64_tr_b16 v[98:99], v162 offset:256\n\t" \
  "ds_read_b64_tr_b16 v[104:105], v163 offset:0\n\t" \
  "ds_read_b64_tr_b16 v[106:107], v163 offset:256\n\t" \
  "ds_read_b64_tr_b16 v[144:145], v168 offset:0\n\t" \
  "ds_read_b64_tr_b16 v[146:147], v168 offset:256\n\t" \
  "ds_read_b64_tr_b16 v[152:153], v169 offset:0\n\t" \
  "ds_read_b64_tr_b16 v[154:155], v169 offset:256\n\t" \
  "s_waitcnt lgkmcnt(4)\n\t" \
  "v_mfma_f32_16x16x32_bf16 v[0:3], v[80:83], v[112:115], v[0:3]\n\t" \
  "ds_read_b128 v[128:131], v166 offset:0\n\t" \
  "s_add_u32 m0, s50, s56\n\t" \
  "v_mfma_f32_16x16x32_bf16 v[4:7], v[80:83], v[116:119], v[4:7]\n\t" \
  "ds_read_b64_tr_b16 v[84:85], v160 offset:8192\n\t" \
  "global_load_lds_dwordx4 v172, s[40:41]\n\t" \
  "v_mfma_f32_16x16x32_bf16 v[8:11], v[80:83], v[120:123], v[8:11]\n\t" \
  "ds_read_b64_tr_b16 v[86:87], v160 offset:8448\n\t" \
  "s_add_u32 m0, m0, 0x400\n\t" \
  "v_mfma_f32_16x16x32_bf16 v[12:15], v[80:83], v[124:127], v[12:15]\n\t" \
  "ds_read_b128 v[132:135], v167 offset:0\n\t" \
  "s_waitcnt lgkmcnt(4)\n\t" \
  "v_mfma_f32_16x16x32_bf16 v[16:19], v[88:91], v[112:115], v[16:19]\n\t" \
  "ds_read_b128 v[136:139], v166 offset:4096\n\t" \
  "global_load_lds_dwordx4 v172, s[42:43]\n\t" \
  "v_mfma_f32_16x16x32_bf16 v[20:23], v[88:91], v[116:119], v[20:23]\n\t" \
  "ds_read_b128 v[140:143], v167 offset:4096\n\t" \
  "s_add_u32 m0, s48, s56\n\t" \
  "v_mfma_f32_16x16x32_bf16 v[24:27], v[88:91], v[120:123], v[24:27]\n\t" \
  "ds_read_b64_tr_b16 v[92:93], v161 offset:8192\n\t" \
  "v_add_u32 v172, 221184, v172\n\t" \
  "v_mfma_f32_16x16x32_bf16 v[28:31], v[88:91], v[124:127], v[28:31]\n\t" \
  "ds_read_b64_tr_b16 v[94:95], v161 offset:8448\n\t" \
  "global_load_lds_dwordx4 v170, s[36:37]\n\t" \
  "v_mfma_f32_16x16x32_bf16 v[32:35], v[96:99], v[112:115], v[32:35]\n\t" \
  "ds_read_b64_tr_b16 v[100:101], v162 offset:8192\n\t" \
  "s_add_u32 m0, m0, 0x400\n\t" \
  "v_mfma_f32_16x16x32_bf16 v[36:39], v[96:99], v[116:119], v[36:39]\n\t" \
  "ds_read_b64_tr_b16 v[102:103], v162 offset:8448\n\t" \
  "global_load_lds_dwordx4 v171, s[36:37]\n\t" \
  "v_mfma_f32_16x16x32_bf16 v[40:43], v[96:99], v[120:123], v[40:43]\n\t" \
  "ds_read_b64_tr_b16 v[108:109], v163 offset:8192\n\t" \
  "v_add_u32 v164, s55, v164\n\t" \
  "v_mfma_f32_16x16x32_bf16 v[44:47], v[96:99], v[124:127], v[44:47]\n\t" \
  "ds_read_b64_tr_b16 v[110:111], v163 offset:8448\n\t" \
  "v_add_u32 v165, s55, v165\n\t" \
  "v_mfma_f32_16x16x32_bf16 v[48:51], v[104:107], v[112:115], v[48:51]\n\t" \
  "ds_read_b64_tr_b16 v[148:149], v168 offset:1024\n\t" \
  "v_add_u32 v160, s55, v160\n\t" \
  "v_mfma_f32_16x16x32_bf16 v[52:55], v[104:107], v[116:119], v[52:55]\n\t" \
  "ds_read_b64_tr_b16 v[150:151], v168 offset:1280\n\t" \
  "v_add_u32 v161, s55, v161\n\t" \
  "v_mfma_f32_16x16x32_bf16 v[56:59], v[104:107], v[120:123], v[56:59]\n\t" \
  "ds_read_b64_tr_b16 v[156:157], v169 offset:1024\n\t" \
  "v_add_u32 v162, s55, v162\n\t" \
  "v_mfma_f32_16x16x32_bf16 v[60:63], v[104:107], v[124:127], v[60:63]\n\t" \
  "ds_read_b64_tr_b16 v[158:159], v169 offset:1280\n\t" \
  "v_add_u32 v163, s55, v163\n\t" \
  "v_mfma_f32_16x16x32_bf16 v[64:67], v[144:147], v[112:115], v[64:67]\n\t" \
  "v_add_u32 v168, s55, v168\n\t" \
  "v_add_u32 v169, s55, v169\n\t" \
  "v_mfma_f32_16x16x32_bf16 v[68:71], v[152:155], v[112:115], v[68:71]\n\t" \
  "s_waitcnt lgkmcnt(4)\n\t" \
  "v_smfmac_f32_16x16x64_bf16 v[72:75], v[176:179], v[80:87], v174\n\t" \
  "v_mfma_f32_16x16x32_bf16 v[0:3], v[84:87], v[128:131], v[0:3]\n\t" \
  "v_mfma_f32_16x16x32_bf16 v[4:7], v[84:87], v[132:135], v[4:7]\n\t" \
  "v_mfma_f32_16x16x32_bf16 v[8:11], v[84:87], v[136:139], v[8:11]\n\t" \
  "s_setprio 1\n\t" \
  "s_waitcnt vmcnt(6) lgkmcnt(0)\n\t" \
  "s_branch .Lg1yf%=\n\t" \
  ".Lg1af%=:\n\t" \
  "v_mfma_f32_16x16x32_bf16 v[0:3], v[80:83], v[112:115], v[0:3]\n\t" \
  "ds_read_b128 v[128:131], v166 offset:0\n\t" \
  "s_setprio 0\n\t" \
  "v_mfma_f32_16x16x32_bf16 v[4:7], v[80:83], v[116:119], v[4:7]\n\t" \
  "ds_read_b64_tr_b16 v[84:85], v160 offset:8192\n\t" \
  "s_add_u32 m0, s50, s56\n\t" \
  "v_mfma_f32_16x16x32_bf16 v[8:11], v[80:83], v[120:123], v[8:11]\n\t" \
  "ds_read_b64_tr_b16 v[86:87], v160 offset:8448\n\t" \
  "global_load_lds_dwordx4 v172, s[40:41]\n\t" \
  "v_mfma_f32_16x16x32_bf16 v[12:15], v[80:83], v[124:127], v[12:15]\n\t" \
  "ds_read_b128 v[132:135], v167 offset:0\n\t" \
  "s_waitcnt lgkmcnt(4)\n\t" \
  "v_mfma_f32_16x16x32_bf16 v[16:19], v[88:91], v[112:115], v[16:19]\n\t" \
  "ds_read_b128 v[136:139], v166 offset:4096\n\t" \
  "s_add_u32 m0, m0, 0x400\n\t" \
  "v_mfma_f32_16x16x32_bf16 v[20:23], v[88:91], v[116:119], v[20:23]\n\t" \
  "ds_read_b128 v[140:143], v167 offset:4096\n\t" \
  "global_load_lds_dwordx4 v172, s[42:43]\n\t" \
  "v_mfma_f32_16x16x32_bf16 v[24:27], v[88:91], v[120:123], v[24:27]\n\t" \
  "ds_read_b64_tr_b16 v[92:93], v161 offset:8192\n\t" \
  "s_add_u32 m0, s48, s56\n\t" \
  "v_mfma_f32_16x16x32_bf16 v[28:31], v[88:91], v[124:127], v[28:31]\n\t" \
  "ds_read_b64_tr_b16 v[94:95], v161 offset:8448\n\t" \
  "v_add_u32 v172, 221184, v172\n\t" \
  "v_mfma_f32_16x16x32_bf16 v[32:35], v[96:99], v[112:115], v[32:35]\n\t" \
  "ds_read_b64_tr_b16 v[100:101], v162 offset:8192\n\t" \
  "global_load_lds_dwordx4 v170, s[36:37]\n\t" \
  "v_mfma_f32_16x16x32_bf16 v[36:39], v[96:99], v[116:119], v[36:39]\n\t" \
  "ds_read_b64_tr_b16 v[102:103], v162 offset:8448\n\t" \
  "s_add_u32 m0, m0, 0x400\n\t" \
  "v_mfma_f32_16x16x32_bf16 v[40:43], v[96:99], v[120:123], v[40:43]\n\t" \
  "ds_read_b64_tr_b16 v[108:109], v163 offset:8192\n\t" \
  "global_load_lds_dwordx4 v171, s[36:37]\n\t" \
  "v_mfma_f32_16x16x32_bf16 v[44:47], v[96:99], v[124:127], v[44:47]\n\t" \
  "ds_read_b64_tr_b16 v[110:111], v163 offset:8448\n\t" \
  "v_add_u32 v164, s55, v164\n\t" \
  "v_mfma_f32_16x16x32_bf16 v[48:51], v[104:107], v[112:115], v[48:51]\n\t" \
  "ds_read_b64_tr_b16 v[148:149], v168 offset:1024\n\t" \
  "v_add_u32 v165, s55, v165\n\t" \
  "v_mfma_f32_16x16x32_bf16 v[52:55], v[104:107], v[116:119], v[52:55]\n\t" \
  "ds_read_b64_tr_b16 v[150:151], v168 offset:1280\n\t" \
  "s_bitcmp1_b32 s52, s54\n\t" \
  "v_mfma_f32_16x16x32_bf16 v[56:59], v[104:107], v[120:123], v[56:59]\n\t" \
  "ds_read_b64_tr_b16 v[156:157], v169 offset:1024\n\t" \
  "v_add_u32 v160, s55, v160\n\t" \
  "v_mfma_f32_16x16x32_bf16 v[60:63], v[104:107], v[124:127], v[60:63]\n\t" \
  "ds_read_b64_tr_b16 v[158:159], v169 offset:1280\n\t" \
  "v_add_u32 v161, s55, v161\n\t" \
  "v_mfma_f32_16x16x32_bf16 v[64:67], v[144:147], v[112:115], v[64:67]\n\t" \
  "v_add_u32 v162, s55, v162\n\t" \
  "v_add_u32 v163, s55, v163\n\t" \
  "v_mfma_f32_16x16x32_bf16 v[68:71], v[152:155], v[112:115], v[68:71]\n\t" \
  "v_add_u32 v168, s55, v168\n\t" \
  "s_waitcnt lgkmcnt(4)\n\t" \
  "v_smfmac_f32_16x16x64_bf16 v[72:75], v[176:179], v[80:87], v174\n\t" \
  "v_add_u32 v169, s55, v169\n\t" \
  "v_mfma_f32_16x16x32_bf16 v[0:3], v[84:87], v[128:131], v[0:3]\n\t" \
  "v_mfma_f32_16x16x32_bf16 v[4:7], v[84:87], v[132:135], v[4:7]\n\t" \
  "v_mfma_f32_16x16x32_bf16 v[8:11], v[84:87], v[136:139], v[8:11]\n\t" \
  "s_setprio 1\n\t" \
  "s_waitcnt vmcnt(4) lgkmcnt(0)\n\t" \
  "s_cbranch_scc1 .Lg1tf%=\n\t" \
  ".Lg1yf%=:\n\t" \
  "s_barrier\n\t" \
  "v_smfmac_f32_16x16x64_bf16 v[76:79], v[176:179], v[144:151], v174\n\t" \
  "ds_read_b128 v[112:115], v164 offset:0\n\t" \
  "v_mfma_f32_16x16x32_bf16 v[12:15], v[84:87], v[140:143], v[12:15]\n\t" \
  "ds_read_b64_tr_b16 v[80:81], v160 offset:0\n\t" \
  "s_add_u32 m0, s49, s56\n\t" \
  "v_mfma_f32_16x16x32_bf16 v[16:19], v[92:95], v[128:131], v[16:19]\n\t" \
  "ds_read_b64_tr_b16 v[82:83], v160 offset:256\n\t" \
  "global_load_lds_dwordx4 v170, s[38:39]\n\t" \
  "v_mfma_f32_16x16x32_bf16 v[20:23], v[92:95], v[132:135], v[20:23]\n\t" \
  "ds_read_b128 v[116:119], v165 offset:0\n\t" \
  "s_add_u32 m0, m0, 0x400\n\t" \
  "v_mfma_f32_16x16x32_bf16 v[24:27], v[92:95], v[136:139], v[24:27]\n\t" \
  "ds_read_b128 v[120:123], v164 offset:4096\n\t" \
  "v_add_u32 v170, 0x80, v170\n\t" \
  "v_mfma_f32_16x16x32_bf16 v[28:31], v[92:95], v[140:143], v[28:31]\n\t" \
  "ds_read_b128 v[124:127], v165 offset:4096\n\t" \
  "global_load_lds_dwordx4 v171, s[38:39]\n\t" \
  "v_mfma_f32_16x16x32_bf16 v[32:35], v[100:103], v[128:131], v[32:35]\n\t" \
  "ds_read_b64_tr_b16 v[88:89], v161 offset:0\n\t" \
  "v_add_u32 v171, 0x80, v171\n\t" \
  "v_mfma_f32_16x16x32_bf16 v[36:39], v[100:103], v[132:135], v[36:39]\n\t" \
  "ds_read_b64_tr_b16 v[90:91], v161 offset:256\n\t" \
  "s_add_u32 m0, s51, s56\n\t" \
  "v_mfma_f32_16x16x32_bf16 v[40:43], v[100:103], v[136:139], v[40:43]\n\t" \
  "ds_read_b64_tr_b16 v[96:97], v162 offset:0\n\t" \
  "s_mov_b32 exec_hi, 0\n\t" \
  "global_load_lds_dwordx4 v173, s[44:45]\n\t" \
  "s_mov_b32 exec_hi, -1\n\t" \
  "v_mfma_f32_16x16x32_bf16 v[44:47], v[100:103], v[140:143], v[44:47]\n\t" \
  "ds_read_b64_tr_b16 v[98:99], v162 offset:256\n\t" \
  "v_add_u32 v173, 221184, v173\n\t" \
  "v_mfma_f32_16x16x32_bf16 v[48:51], v[108:111], v[128:131], v[48:51]\n\t" \
  "ds_read_b64_tr_b16 v[104:105], v163 offset:0\n\t" \
  "v_add_u32 v166, s55, v166\n\t" \
  "v_mfma_f32_16x16x32_bf16 v[52:55], v[108:111], v[132:135], v[52:55]\n\t" \
  "ds_read_b64_tr_b16 v[106:107], v163 offset:256\n\t" \
  "v_add_u32 v167, s55, v167\n\t" \
  "v_mfma_f32_16x16x32_bf16 v[56:59], v[108:111], v[136:139], v[56:59]\n\t" \
  "ds_read_b64_tr_b16 v[144:145], v168 offset:0\n\t" \
  "s_mov_b32 s56, s53\n\t" \
  "v_mfma_f32_16x16x32_bf16 v[60:63], v[108:111], v[140:143], v[60:63]\n\t" \
  "ds_read_b64_tr_b16 v[146:147], v168 offset:256\n\t" \
  "s_add_u32 s53, s53, s55\n\t" \
  "v_mfma_f32_16x16x32_bf16 v[64:67], v[148:151], v[128:131], v[64:67]\n\t" \
  "ds_read_b64_tr_b16 v[152:153], v169 offset:0\n\t" \
  "s_add_u32 s54, s54, 1\n\t" \
  "v_mfma_f32_16x16x32_bf16 v[68:71], v[156:159], v[128:131], v[68:71]\n\t" \
  "ds_read_b64_tr_b16 v[154:155], v169 offset:256\n\t" \
  "s_cmp_eq_u32 s53, 106496\n\t" \
  "s_cselect_b32 s55, s66, s67\n\t" \
  "s_waitcnt lgkmcnt(4)\n\t" \
  "s_branch .Lg1af%=\n\t" \
  ".Lg1p%=:\n\t" \
  "global_store_dwordx4 v237, v[188:191], s[6:7] sc1\n\t" \
  "s_mov_b32 exec_hi, 0xffff\n\t" \
  "global_store_dwordx4 v237, v[188:191], s[6:7] offset:1024 sc1\n\t" \
  "s_mov_b32 exec_hi, -1\n\t" \
  "s_branch .Lg1yf%=\n\t" \
  ".Lg1tf%=:\n\t" \
  "s_cmp_lt_u32 s54, 5\n\t" \
  "s_cbranch_scc1 .Lg1p%=\n\t" \
  "s_barrier\n\t" \
  "s_cmp_ge_u32 s32, 4\n\t" \
  "s_cbranch_scc1 .Lg%=\n\t" \
  "v_smfmac_f32_16x16x64_bf16 v[76:79], v[176:179], v[144:151], v174\n\t" \
  "ds_read_b128 v[112:115], v164 offset:0\n\t" \
  "v_mfma_f32_16x16x32_bf16 v[12:15], v[84:87], v[140:143], v[12:15]\n\t" \
  "ds_read_b64_tr_b16 v[80:81], v160 offset:0\n\t" \
  "s_add_u32 m0, s49, s56\n\t" \
  "v_mfma_f32_16x16x32_bf16 v[16:19], v[92:95], v[128:131], v[16:19]\n\t" \
  "ds_read_b64_tr_b16 v[82:83], v160 offset:256\n\t" \
  "global_load_lds_dwordx4 v170, s[38:39]\n\t" \
  "v_mfma_f32_16x16x32_bf16 v[20:23], v[92:95], v[132:135], v[20:23]\n\t" \
  "ds_read_b128 v[116:119], v165 offset:0\n\t" \
  "s_add_u32 m0, m0, 0x400\n\t" \
  "v_mfma_f32_16x16x32_bf16 v[24:27], v[92:95], v[136:139], v[24:27]\n\t" \
  "ds_read_b128 v[120:123], v164 offset:4096\n\t" \
  "global_load_lds_dwordx4 v171, s[38:39]\n\t" \
  "v_mfma_f32_16x16x32_bf16 v[28:31], v[92:95], v[140:143], v[28:31]\n\t" \
  "ds_read_b128 v[124:127], v165 offset:4096\n\t" \
  "s_add_u32 m0, s51, s56\n\t" \
  "v_mfma_f32_16x16x32_bf16 v[32:35], v[100:103], v[128:131], v[32:35]\n\t" \
  "ds_read_b64_tr_b16 v[88:89], v161 offset:0\n\t" \
  "s_mov_b32 exec_hi, 0\n\t" \
  "global_load_lds_dwordx4 v173, s[44:45]\n\t" \
  "s_mov_b32 exec_hi, -1\n\t" \
  "v_mfma_f32_16x16x32_bf16 v[36:39], v[100:103], v[132:135], v[36:39]\n\t" \
  "ds_read_b64_tr_b16 v[90:91], v161 offset:256\n\t" \
  "v_add_u32 v166, s55, v166\n\t" \
  "v_mfma_f32_16x16x32_bf16 v[40:43], v[100:103], v[136:139], v[40:43]\n\t" \
  "ds_read_b64_tr_b16 v[96:97], v162 offset:0\n\t" \
  "v_add_u32 v167, s55, v167\n\t" \
  "v_mfma_f32_16x16x32_bf16 v[44:47], v[100:103], v[140:143], v[44:47]\n\t" \
  "ds_read_b64_tr_b16 v[98:99], v162 offset:256\n\t" \
  "s_mov_b32 s55, s66\n\t" \
  "v_mfma_f32_16x16x32_bf16 v[48:51], v[108:111], v[128:131], v[48:51]\n\t" \
  "ds_read_b64_tr_b16 v[104:105], v163 offset:0\n\t" \
  "v_lshl_add_u32 v175, s32, 6, v229\n\t" \
  "v_mfma_f32_16x16x32_bf16 v[52:55], v[108:111], v[132:135], v[52:55]\n\t" \
  "ds_read_b64_tr_b16 v[106:107], v163 offset:256\n\t" \
  "v_xor_b32 v232, 64, v175\n\t" \
  "v_mfma_f32_16x16x32_bf16 v[56:59], v[108:111], v[136:139], v[56:59]\n\t" \
  "ds_read_b64_tr_b16 v[144:145], v168 offset:0\n\t" \
  "v_xor_b32 v172, 128, v175\n\t" \
  "v_mfma_f32_16x16x32_bf16 v[60:63], v[108:111], v[140:143], v[60:63]\n\t" \
  "ds_read_b64_tr_b16 v[146:147], v168 offset:256\n\t" \
  "v_xor_b32 v173, 192, v175\n\t" \
  "v_mfma_f32_16x16x32_bf16 v[64:67], v[148:151], v[128:131], v[64:67]\n\t" \
  "ds_read_b64_tr_b16 v[152:153], v169 offset:0\n\t" \
  "v_mfma_f32_16x16x32_bf16 v[68:71], v[156:159], v[128:131], v[68:71]\n\t" \
  "ds_read_b64_tr_b16 v[154:155], v169 offset:256\n\t" \
  "s_waitcnt lgkmcnt(4)\n\t" \
  "v_mfma_f32_16x16x32_bf16 v[0:3], v[80:83], v[112:115], v[0:3]\n\t" \
  "ds_read_b128 v[128:131], v166 offset:0\n\t" \
  "s_setprio 0\n\t" \
  "v_mfma_f32_16x16x32_bf16 v[4:7], v[80:83], v[116:119], v[4:7]\n\t" \
  "ds_read_b64_tr_b16 v[84:85], v160 offset:8192\n\t" \
  "global_load_dwordx4 v[240:243], v180, s[46:47] sc1\n\t" \
  "v_mfma_f32_16x16x32_bf16 v[8:11], v[80:83], v[120:123], v[8:11]\n\t" \
  "ds_read_b64_tr_b16 v[86:87], v160 offset:8448\n\t" \
  "global_load_dwordx4 v[244:247], v181, s[46:47] sc1\n\t" \
  "v_mfma_f32_16x16x32_bf16 v[12:15], v[80:83], v[124:127], v[12:15]\n\t" \
  "ds_read_b128 v[132:135], v167 offset:0\n\t" \
  "s_waitcnt lgkmcnt(4)\n\t" \
  "v_mfma_f32_16x16x32_bf16 v[16:19], v[88:91], v[112:115], v[16:19]\n\t" \
  "ds_read_b128 v[136:139], v166 offset:4096\n\t" \
  "global_load_dwordx4 v[248:251], v182, s[46:47] sc1\n\t" \
  "v_mfma_f32_16x16x32_bf16 v[20:23], v[88:91], v[116:119], v[20:23]\n\t" \
  "ds_read_b128 v[140:143], v167 offset:4096\n\t" \
  "global_load_dwordx4 v[252:255], v183, s[46:47] sc1\n\t" \
  "v_mfma_f32_16x16x32_bf16 v[24:27], v[88:91], v[120:123], v[24:27]\n\t" \
  "ds_read_b64_tr_b16 v[92:93], v161 offset:8192\n\t" \
  "v_add_u32 v164, s55, v164\n\t" \
  "v_mfma_f32_16x16x32_bf16 v[28:31], v[88:91], v[124:127], v[28:31]\n\t" \
  "ds_read_b64_tr_b16 v[94:95], v161 offset:8448\n\t" \
  "v_add_u32 v165, s55, v165\n\t" \
  "v_mfma_f32_16x16x32_bf16 v[32:35], v[96:99], v[112:115], v[32:35]\n\t" \
  "ds_read_b64_tr_b16 v[100:101], v162 offset:8192\n\t" \
  "v_add_u32 v184, 0, v229\n\t" \
  "v_mfma_f32_16x16x32_bf16 v[36:39], v[96:99], v[116:119], v[36:39]\n\t" \
  "ds_read_b64_tr_b16 v[102:103], v162 offset:8448\n\t" \
  "v_xor_b32 v185, 64, v184\n\t" \
  "v_mfma_f32_16x16x32_bf16 v[40:43], v[96:99], v[120:123], v[40:43]\n\t" \
  "ds_read_b64_tr_b16 v[108:109], v163 offset:8192\n\t" \
  "s_mul_i32 s64, s34, 5120\n\t" \
  "v_mfma_f32_16x16x32_bf16 v[44:47], v[96:99], v[124:127], v[44:47]\n\t" \
  "ds_read_b64_tr_b16 v[110:111], v163 offset:8448\n\t" \
  "s_add_u32 s64, s64, 1024\n\t" \
  "v_mfma_f32_16x16x32_bf16 v[48:51], v[104:107], v[112:115], v[48:51]\n\t" \
  "ds_read_b64_tr_b16 v[148:149], v168 offset:1024\n\t" \
  "v_add_u32 v230, s64, v237\n\t" \
  "v_mfma_f32_16x16x32_bf16 v[52:55], v[104:107], v[116:119], v[52:55]\n\t" \
  "ds_read_b64_tr_b16 v[150:151], v168 offset:1280\n\t" \
  "v_lshl_add_u32 v187, s32, 6, v192\n\t" \
  "v_mfma_f32_16x16x32_bf16 v[56:59], v[104:107], v[120:123], v[56:59]\n\t" \
  "ds_read_b64_tr_b16 v[156:157], v169 offset:1024\n\t" \
  "s_mul_i32 s63, s32, 57344\n\t" \
  "v_mfma_f32_16x16x32_bf16 v[60:63], v[104:107], v[124:127], v[60:63]\n\t" \
  "ds_read_b64_tr_b16 v[158:159], v169 offset:1280\n\t" \
  "s_add_u32 s63, s63, 589824\n\t" \
  "v_mfma_f32_16x16x32_bf16 v[64:67], v[144:147], v[112:115], v[64:67]\n\t" \
  "s_add_u32 s36, s24, s63\n\t" \
  "s_addc_u32 s37, s25, 0\n\t" \
  "v_mfma_f32_16x16x32_bf16 v[68:71], v[152:155], v[112:115], v[68:71]\n\t" \
  "v_lshrrev_b32 v238, 1, v237\n\t" \
  "s_waitcnt lgkmcnt(4)\n\t" \
  "v_smfmac_f32_16x16x64_bf16 v[72:75], v[176:179], v[80:87], v174\n\t" \
  "s_lshl2_add_u32 s63, s62, s32\n\t" \
  "v_mfma_f32_16x16x32_bf16 v[0:3], v[84:87], v[128:131], v[0:3]\n\t" \
  "s_mul_i32 s63, s63, 2560\n\t" \
  "s_add_u32 s63, s63, 819200\n\t" \
  "v_mfma_f32_16x16x32_bf16 v[4:7], v[84:87], v[132:135], v[4:7]\n\t" \
  "s_add_u32 s38, s24, s63\n\t" \
  "s_addc_u32 s39, s25, 0\n\t" \
  "v_mfma_f32_16x16x32_bf16 v[8:11], v[84:87], v[136:139], v[8:11]\n\t" \
  "v_add_u32 v160, s55, v160\n\t" \
  "v_add_u32 v161, s55, v161\n\t" \
  "v_add_u32 v162, s55, v162\n\t" \
  "v_add_u32 v163, s55, v163\n\t" \
  "v_add_u32 v168, s55, v168\n\t" \
  "v_add_u32 v169, s55, v169\n\t" \
  "s_setprio 1\n\t" \
  "s_waitcnt vmcnt(4) lgkmcnt(0)\n\t" \
  "s_barrier\n\t" \
  "v_smfmac_f32_16x16x64_bf16 v[76:79], v[176:179], v[144:151], v174\n\t" \
  "ds_read_b128 v[112:115], v164 offset:0\n\t" \
  "v_mfma_f32_16x16x32_bf16 v[12:15], v[84:87], v[140:143], v[12:15]\n\t" \
  "ds_read_b64_tr_b16 v[80:81], v160 offset:0\n\t" \
  "v_add_u32 v166, s55, v166\n\t" \
  "v_mfma_f32_16x16x32_bf16 v[16:19], v[92:95], v[128:131], v[16:19]\n\t" \
  "ds_read_b64_tr_b16 v[82:83], v160 offset:256\n\t" \
  "v_add_u32 v167, s55, v167\n\t" \
  "v_mfma_f32_16x16x32_bf16 v[20:23], v[92:95], v[132:135], v[20:23]\n\t" \
  "ds_read_b128 v[116:119], v165 offset:0\n\t" \
  "v_mov_b64 v[212:213], 0\n\t" \
  "v_mfma_f32_16x16x32_bf16 v[24:27], v[92:95], v[136:139], v[24:27]\n\t" \
  "ds_read_b128 v[120:123], v164 offset:4096\n\t" \
  "v_and_b32 v214, 31, v228\n\t" \
  "v_mfma_f32_16x16x32_bf16 v[28:31], v[92:95], v[140:143], v[28:31]\n\t" \
  "ds_read_b128 v[124:127], v165 offset:4096\n\t" \
  "v_lshrrev_b32 v215, 5, v228\n\t" \
  "v_mfma_f32_16x16x32_bf16 v[32:35], v[100:103], v[128:131], v[32:35]\n\t" \
  "ds_read_b64_tr_b16 v[88:89], v161 offset:0\n\t" \
  "v_mul_u32_u24 v239, 336, v214\n\t" \
  "v_mfma_f32_16x16x32_bf16 v[36:39], v[100:103], v[132:135], v[36:39]\n\t" \
  "ds_read_b64_tr_b16 v[90:91], v161 offset:256\n\t" \
  "v_lshl_add_u32 v239, v215, 7, v239\n\t" \
  "v_mfma_f32_16x16x32_bf16 v[40:43], v[100:103], v[136:139], v[40:43]\n\t" \
  "ds_read_b64_tr_b16 v[96:97], v162 offset:0\n\t" \
  "v_add_u32 v239, 65536, v239\n\t" \
  "v_mfma_f32_16x16x32_bf16 v[44:47], v[100:103], v[140:143], v[44:47]\n\t" \
  "ds_read_b64_tr_b16 v[98:99], v162 offset:256\n\t" \
  "v_mul_u32_u24 v214, 80, v214\n\t" \
  "v_mfma_f32_16x16x32_bf16 v[48:51], v[108:111], v[128:131], v[48:51]\n\t" \
  "ds_read_b64_tr_b16 v[104:105], v163 offset:0\n\t" \
  "s_mul_i32 s63, s32, 2560\n\t" \
  "v_mfma_f32_16x16x32_bf16 v[52:55], v[108:111], v[132:135], v[52:55]\n\t" \
  "ds_read_b64_tr_b16 v[106:107], v163 offset:256\n\t" \
  "s_add_u32 s64, s63, 21504\n\t" \
  "v_mfma_f32_16x16x32_bf16 v[56:59], v[108:111], v[136:139], v[56:59]\n\t" \
  "ds_read_b64_tr_b16 v[144:145], v168 offset:0\n\t" \
  "v_lshl_add_u32 v215, v215, 4, s64\n\t" \
  "v_mfma_f32_16x16x32_bf16 v[60:63], v[108:111], v[140:143], v[60:63]\n\t" \
  "ds_read_b64_tr_b16 v[146:147], v168 offset:256\n\t" \
  "v_mov_b32 v236, 0xdddddddd\n\t" \
  "v_mfma_f32_16x16x32_bf16 v[64:67], v[148:151], v[128:131], v[64:67]\n\t" \
  "ds_read_b64_tr_b16 v[152:153], v169 offset:0\n\t" \
  "s_mul_i32 s63, s32, 2688\n\t" \
  "v_mfma_f32_16x16x32_bf16 v[68:71], v[156:159], v[128:131], v[68:71]\n\t" \
  "ds_read_b64_tr_b16 v[154:155], v169 offset:256\n\t" \
  "s_add_u32 s63, s63, 65536\n\t" \
  "global_load_dwordx2 v[234:235], v233, s[30:31] sc1\n\t" \
  "s_waitcnt lgkmcnt(4)\n\t" \
  "v_mfma_f32_16x16x32_bf16 v[0:3], v[80:83], v[112:115], v[0:3]\n\t" \
  "ds_read_b128 v[128:131], v166 offset:0\n\t" \
  "s_setprio 0\n\t" \
  "v_mfma_f32_16x16x32_bf16 v[4:7], v[80:83], v[116:119], v[4:7]\n\t" \
  "ds_read_b64_tr_b16 v[84:85], v160 offset:8192\n\t" \
  "v_mul_u32_u24 v218, 13, v228\n\t" \
  "v_mfma_f32_16x16x32_bf16 v[8:11], v[80:83], v[120:123], v[8:11]\n\t" \
  "ds_read_b64_tr_b16 v[86:87], v160 offset:8448\n\t" \
  "v_lshrrev_b32 v218, 8, v218\n\t" \
  "v_mfma_f32_16x16x32_bf16 v[12:15], v[80:83], v[124:127], v[12:15]\n\t" \
  "ds_read_b128 v[132:135], v167 offset:0\n\t" \
  "s_waitcnt lgkmcnt(4)\n\t" \
  "v_mfma_f32_16x16x32_bf16 v[16:19], v[88:91], v[112:115], v[16:19]\n\t" \
  "ds_read_b128 v[136:139], v166 offset:4096\n\t" \
  "v_add_u32 v218, v218, v228\n\t" \
  "v_mfma_f32_16x16x32_bf16 v[20:23], v[88:91], v[116:119], v[20:23]\n\t" \
  "ds_read_b128 v[140:143], v167 offset:4096\n\t" \
  "v_lshl_add_u32 v218, v218, 4, s63\n\t" \
  "v_mfma_f32_16x16x32_bf16 v[24:27], v[88:91], v[120:123], v[24:27]\n\t" \
  "ds_read_b64_tr_b16 v[92:93], v161 offset:8192\n\t" \
  "v_and_b32 v195, 3, v228\n\t" \
  "v_mfma_f32_16x16x32_bf16 v[28:31], v[88:91], v[124:127], v[28:31]\n\t" \
  "ds_read_b64_tr_b16 v[94:95], v161 offset:8448\n\t" \
  "v_mul_u32_u24 v194, 10304, v195\n\t" \
  "v_mfma_f32_16x16x32_bf16 v[32:35], v[96:99], v[112:115], v[32:35]\n\t" \
  "ds_read_b64_tr_b16 v[100:101], v162 offset:8192\n\t" \
  "v_add_u32 v194, 62720, v194\n\t" \
  "v_mfma_f32_16x16x32_bf16 v[36:39], v[96:99], v[116:119], v[36:39]\n\t" \
  "ds_read_b64_tr_b16 v[102:103], v162 offset:8448\n\t" \
  "v_mul_u32_u24 v195, 2080, v195\n\t" \
  "v_mfma_f32_16x16x32_bf16 v[40:43], v[96:99], v[120:123], v[40:43]\n\t" \
  "ds_read_b64_tr_b16 v[108:109], v163 offset:8192\n\t" \
  "s_waitcnt vmcnt(1)\n\t" \
  "v_mfma_f32_16x16x32_bf16 v[44:47], v[96:99], v[124:127], v[44:47]\n\t" \
  "ds_read_b64_tr_b16 v[110:111], v163 offset:8448\n\t" \
  "v_xor_b32 v188, s28, v241\n\t" \
  "v_mfma_f32_16x16x32_bf16 v[48:51], v[104:107], v[112:115], v[48:51]\n\t" \
  "ds_read_b64_tr_b16 v[148:149], v168 offset:1024\n\t" \
  "v_xor_b32 v189, s28, v243\n\t" \
  "v_mfma_f32_16x16x32_bf16 v[52:55], v[104:107], v[116:119], v[52:55]\n\t" \
  "ds_read_b64_tr_b16 v[150:151], v168 offset:1280\n\t" \
  "v_bitop3_b32 v188, v188, s28, v245 bitop3:0xf6\n\t" \
  "v_mfma_f32_16x16x32_bf16 v[56:59], v[104:107], v[120:123], v[56:59]\n\t" \
  "ds_read_b64_tr_b16 v[156:157], v169 offset:1024\n\t" \
  "v_bitop3_b32 v189, v189, s28, v247 bitop3:0xf6\n\t" \
  "v_mfma_f32_16x16x32_bf16 v[60:63], v[104:107], v[124:127], v[60:63]\n\t" \
  "ds_read_b64_tr_b16 v[158:159], v169 offset:1280\n\t" \
  "v_bitop3_b32 v188, v188, s28, v249 bitop3:0xf6\n\t" \
  "v_mfma_f32_16x16x32_bf16 v[64:67], v[144:147], v[112:115], v[64:67]\n\t" \
  "v_bitop3_b32 v189, v189, s28, v251 bitop3:0xf6\n\t" \
  "v_bitop3_b32 v188, v188, s28, v253 bitop3:0xf6\n\t" \
  "v_mfma_f32_16x16x32_bf16 v[68:71], v[152:155], v[112:115], v[68:71]\n\t" \
  "v_bitop3_b32 v189, v189, s28, v255 bitop3:0xf6\n\t" \
  "s_waitcnt lgkmcnt(4)\n\t" \
  "v_smfmac_f32_16x16x64_bf16 v[72:75], v[176:179], v[80:87], v174\n\t" \
  "v_or_b32 v188, v188, v189\n\t" \
  "v_mfma_f32_16x16x32_bf16 v[0:3], v[84:87], v[128:131], v[0:3]\n\t" \
  "v_cmp_ne_u32 vcc, 0, v188\n\t" \
  "v_add_u32 v195, 112256, v195\n\t" \
  "v_mfma_f32_16x16x32_bf16 v[4:7], v[84:87], v[132:135], v[4:7]\n\t" \
  "s_cbranch_vccnz .Lstaa%=\n\t" \
  ".Lstab%=:\n\t" \
  "v_mfma_f32_16x16x32_bf16 v[8:11], v[84:87], v[136:139], v[8:11]\n\t" \
  "s_setprio 1\n\t" \
  "s_waitcnt lgkmcnt(0)\n\t" \
  "s_barrier\n\t" \
  "v_smfmac_f32_16x16x64_bf16 v[76:79], v[176:179], v[144:151], v174\n\t" \
  "s_waitcnt vmcnt(0)\n\t" \
  "v_mfma_f32_16x16x32_bf16 v[12:15], v[84:87], v[140:143], v[12:15]\n\t" \
  "v_cmp_ne_u64 vcc, s[28:29], v[234:235]\n\t" \
  "v_mul_f32 v242, v242, v242\n\t" \
  "v_mfma_f32_16x16x32_bf16 v[16:19], v[92:95], v[128:131], v[16:19]\n\t" \
  "v_mul_f32 v246, v246, v246\n\t" \
  "s_mov_b32 s42, 1\n\t" \
  "s_cbranch_vccnz .Lano%=\n\t" \
  "s_mov_b32 exec_hi, 0xfffffff\n\t" \
  "global_load_dwordx4 v[160:163], v237, s[38:39] offset:0 sc1\n\t" \
  "global_load_dwordx4 v[164:167], v237, s[38:39] offset:960 sc1\n\t" \
  "global_load_dwordx4 v[168:171], v237, s[38:39] offset:1600 sc1\n\t" \
  "s_mov_b32 exec_hi, -1\n\t" \
  "global_load_dwordx4 v[208:211], v237, s[36:37] offset:0 sc1\n\t" \
  "global_load_dwordx2 v[216:217], v238, s[36:37] offset:3072 sc1\n\t" \
  "global_load_dwordx4 v[220:223], v237, s[36:37] offset:1024 sc1\n\t" \
  "global_load_dwordx4 v[224:227], v237, s[36:37] offset:2048 sc1\n\t" \
  ".Layes%=:\n\t" \
  "v_mfma_f32_16x16x32_bf16 v[20:23], v[92:95], v[132:135], v[20:23]\n\t" \
  "v_mul_f32 v250, v250, v250\n\t" \
  "v_mul_f32 v254, v254, v254\n\t" \
  "v_mfma_f32_16x16x32_bf16 v[24:27], v[92:95], v[136:139], v[24:27]\n\t" \
  "v_add_f32 v72, v72, v73\n\t" \
  "v_add_f32 v76, v76, v77\n\t" \
  "v_mfma_f32_16x16x32_bf16 v[28:31], v[92:95], v[140:143], v[28:31]\n\t" \
  "v_mov_b32 v73, 0\n\t" \
  "s_mov_b64 exec, 0xffff\n\t" \
  "ds_write_b32 v187, v72 offset:0\n\t" \
  "ds_write_b32 v192, v76 offset:512\n\t" \
  "ds_write_b32 v192, v73 offset:768\n\t" \
  "s_mov_b64 exec, -1\n\t" \
  "v_mfma_f32_16x16x32_bf16 v[32:35], v[100:103], v[128:131], v[32:35]\n\t" \
  "v_add_u32 v241, v214, v215\n\t" \
  "v_add_u32 v243, 41216, v241\n\t" \
  "v_mfma_f32_16x16x32_bf16 v[36:39], v[100:103], v[132:135], v[36:39]\n\t" \
  "v_mov_b32 v245, 804\n\t" \
  "v_mov_b32 v247, 1\n\t" \
  "v_mfma_f32_16x16x32_bf16 v[40:43], v[100:103], v[136:139], v[40:43]\n\t" \
  "v_bfe_u32 v196, v228, 2, 2\n\t" \
  "v_lshrrev_b32 v197, 4, v228\n\t" \
  "v_mfma_f32_16x16x32_bf16 v[44:47], v[100:103], v[140:143], v[44:47]\n\t" \
  "v_lshl_or_b32 v196, v197, 3, v196\n\t" \
  "s_lshl_b32 s65, s32, 5\n\t" \
  "v_mfma_f32_16x16x32_bf16 v[48:51], v[108:111], v[128:131], v[48:51]\n\t" \
  "v_add_u32 v197, s65, v196\n\t" \
  "v_lshl_add_u32 v249, v197, 4, v194\n\t" \
  "v_mfma_f32_16x16x32_bf16 v[52:55], v[108:111], v[132:135], v[52:55]\n\t" \
  "v_mul_u32_u24 v197, 205, v197\n\t" \
  "v_lshrrev_b32 v251, 10, v197\n\t" \
  "v_mfma_f32_16x16x32_bf16 v[56:59], v[108:111], v[136:139], v[56:59]\n\t" \
  "v_add_u32 v197, 820, v197\n\t" \
  "v_lshrrev_b32 v253, 10, v197\n\t" \
  "v_mfma_f32_16x16x32_bf16 v[60:63], v[108:111], v[140:143], v[60:63]\n\t" \
  "v_lshl_add_u32 v251, v251, 4, v195\n\t" \
  "v_lshl_add_u32 v253, v253, 4, v195\n\t" \
  "v_mfma_f32_16x16x32_bf16 v[64:67], v[148:151], v[128:131], v[64:67]\n\t" \
  "v_mfma_f32_16x16x32_bf16 v[68:71], v[156:159], v[128:131], v[68:71]\n\t" \
  "s_setprio 0\n\t" \
  "s_waitcnt lgkmcnt(0)\n\t" \
  "s_barrier\n\t" \
  "ds_read_b128 v[128:131], v175 offset:0\n\t" \
  "ds_read_b128 v[132:135], v232 offset:0\n\t" \
  "ds_read_b128 v[136:139], v172 offset:0\n\t" \
  "ds_read_b128 v[140:143], v173 offset:0\n\t" \
  "ds_read_b128 v[144:147], v184 offset:512\n\t" \
  "ds_read_b128 v[148:151], v185 offset:512\n\t" \
  "s_cmp_eq_u32 s42, 1\n\t" \
  "s_cbranch_scc1 .Lglu%=\n\t" \
  "s_waitcnt vmcnt(0)\n\t" \
  "v_cmp_ne_u64 vcc, s[28:29], v[234:235]\n\t" \
  "s_nop 1\n\t" \
  "s_cbranch_vccnz .Lglu%=\n\t" \
  "s_mov_b32 s42, 1\n\t" \
  "s_mov_b32 exec_hi, 0xfffffff\n\t" \
  "global_load_dwordx4 v[160:163], v237, s[38:39] offset:0 sc1\n\t" \
  "global_load_dwordx4 v[164:167], v237, s[38:39] offset:960 sc1\n\t" \
  "global_load_dwordx4 v[168:171], v237, s[38:39] offset:1600 sc1\n\t" \
  "s_mov_b32 exec_hi, -1\n\t" \
  "global_load_dwordx4 v[208:211], v237, s[36:37] offset:0 sc1\n\t" \
  "global_load_dwordx2 v[216:217], v238, s[36:37] offset:3072 sc1\n\t" \
  "global_load_dwordx4 v[220:223], v237, s[36:37] offset:1024 sc1\n\t" \
  "global_load_dwordx4 v[224:227], v237, s[36:37] offset:2048 sc1\n\t" \
  ".Lglu%=:\n\t" \
  "s_setprio 3\n\t" \
  "s_waitcnt lgkmcnt(5)\n\t" \
  "v_pk_fma_f32 v[0:1], v[240:241], v[128:129], v[0:1] op_sel_hi:[0,1,1] neg_lo:[1,0,0] neg_hi:[1,0,0]\n\t" \
  "v_pk_fma_f32 v[2:3], v[240:241], v[130:131], v[2:3] op_sel_hi:[0,1,1] neg_lo:[1,0,0] neg_hi:[1,0,0]\n\t" \
  "v_pk_fma_f32 v[4:5], v[244:245], v[128:129], v[4:5] op_sel_hi:[0,1,1] neg_lo:[1,0,0] neg_hi:[1,0,0]\n\t" \
  "v_pk_fma_f32 v[6:7], v[244:245], v[130:131], v[6:7] op_sel_hi:[0,1,1] neg_lo:[1,0,0] neg_hi:[1,0,0]\n\t" \
  "v_pk_fma_f32 v[8:9], v[248:249], v[128:129], v[8:9] op_sel_hi:[0,1,1] neg_lo:[1,0,0] neg_hi:[1,0,0]\n\t" \
  "v_pk_fma_f32 v[10:11], v[248:249], v[130:131], v[10:11] op_sel_hi:[0,1,1] neg_lo:[1,0,0] neg_hi:[1,0,0]\n\t" \
  "v_pk_fma_f32 v[12:13], v[252:253], v[128:129], v[12:13] op_sel_hi:[0,1,1] neg_lo:[1,0,0] neg_hi:[1,0,0]\n\t" \
  "v_pk_fma_f32 v[14:15], v[252:253], v[130:131], v[14:15] op_sel_hi:[0,1,1] neg_lo:[1,0,0] neg_hi:[1,0,0]\n\t" \
  "s_waitcnt lgkmcnt(4)\n\t" \
  "v_pk_fma_f32 v[16:17], v[240:241], v[132:133], v[16:17] op_sel_hi:[0,1,1] neg_lo:[1,0,0] neg_hi:[1,0,0]\n\t" \
  "v_pk_fma_f32 v[18:19], v[240:241], v[134:135], v[18:19] op_sel_hi:[0,1,1] neg_lo:[1,0,0] neg_hi:[1,0,0]\n\t" \
  "v_pk_fma_f32 v[20:21], v[244:245], v[132:133], v[20:21] op_sel_hi:[0,1,1] neg_lo:[1,0,0] neg_hi:[1,0,0]\n\t" \
  "v_pk_fma_f32 v[22:23], v[244:245], v[134:135], v[22:23] op_sel_hi:[0,1,1] neg_lo:[1,0,0] neg_hi:[1,0,0]\n\t" \
  "v_pk_fma_f32 v[24:25], v[248:249], v[132:133], v[24:25] op_sel_hi:[0,1,1] neg_lo:[1,0,0] neg_hi:[1,0,0]\n\t" \
  "v_pk_fma_f32 v[26:27], v[248:249], v[134:135], v[26:27] op_sel_hi:[0,1,1] neg_lo:[1,0,0] neg_hi:[1,0,0]\n\t" \
  "v_pk_fma_f32 v[28:29], v[252:253], v[132:133], v[28:29] op_sel_hi:[0,1,1] neg_lo:[1,0,0] neg_hi:[1,0,0]\n\t" \
  "v_pk_fma_f32 v[30:31], v[252:253], v[134:135], v[30:31] op_sel_hi:[0,1,1] neg_lo:[1,0,0] neg_hi:[1,0,0]\n\t" \
  "s_waitcnt lgkmcnt(3)\n\t" \
  "v_pk_fma_f32 v[32:33], v[240:241], v[136:137], v[32:33] op_sel_hi:[0,1,1] neg_lo:[1,0,0] neg_hi:[1,0,0]\n\t" \
  "v_pk_fma_f32 v[34:35], v[240:241], v[138:139], v[34:35] op_sel_hi:[0,1,1] neg_lo:[1,0,0] neg_hi:[1,0,0]\n\t" \
  "v_pk_fma_f32 v[36:37], v[244:245], v[136:137], v[36:37] op_sel_hi:[0,1,1] neg_lo:[1,0,0] neg_hi:[1,0,0]\n\t" \
  "v_pk_fma_f32 v[38:39], v[244:245], v[138:139], v[38:39] op_sel_hi:[0,1,1] neg_lo:[1,0,0] neg_hi:[1,0,0]\n\t" \
  "v_pk_fma_f32 v[40:41], v[248:249], v[136:137], v[40:41] op_sel_hi:[0,1,1] neg_lo:[1,0,0] neg_hi:[1,0,0]\n\t" \
  "v_pk_fma_f32 v[42:43], v[248:249], v[138:139], v[42:43] op_sel_hi:[0,1,1] neg_lo:[1,0,0] neg_hi:[1,0,0]\n\t" \
  "v_pk_fma_f32 v[44:45], v[252:253], v[136:137], v[44:45] op_sel_hi:[0,1,1] neg_lo:[1,0,0] neg_hi:[1,0,0]\n\t" \
  "v_pk_fma_f32 v[46:47], v[252:253], v[138:139], v[46:47] op_sel_hi:[0,1,1] neg_lo:[1,0,0] neg_hi:[1,0,0]\n\t" \
  "s_waitcnt lgkmcnt(2)\n\t" \
  "v_pk_fma_f32 v[48:49], v[240:241], v[140:141], v[48:49] op_sel_hi:[0,1,1] neg_lo:[1,0,0] neg_hi:[1,0,0]\n\t" \
  "v_pk_fma_f32 v[50:51], v[240:241], v[142:143], v[50:51] op_sel_hi:[0,1,1] neg_lo:[1,0,0] neg_hi:[1,0,0]\n\t" \
  "v_pk_fma_f32 v[52:53], v[244:245], v[140:141], v[52:53] op_sel_hi:[0,1,1] neg_lo:[1,0,0] neg_hi:[1,0,0]\n\t" \
  "v_pk_fma_f32 v[54:55], v[244:245], v[142:143], v[54:55] op_sel_hi:[0,1,1] neg_lo:[1,0,0] neg_hi:[1,0,0]\n\t" \
  "v_pk_fma_f32 v[56:57], v[248:249], v[140:141], v[56:57] op_sel_hi:[0,1,1] neg_lo:[1,0,0] neg_hi:[1,0,0]\n\t" \
  "v_pk_fma_f32 v[58:59], v[248:249], v[142:143], v[58:59] op_sel_hi:[0,1,1] neg_lo:[1,0,0] neg_hi:[1,0,0]\n\t" \
  "v_pk_fma_f32 v[60:61], v[252:253], v[140:141], v[60:61] op_sel_hi:[0,1,1] neg_lo:[1,0,0] neg_hi:[1,0,0]\n\t" \
  "v_pk_fma_f32 v[62:63], v[252:253], v[142:143], v[62:63] op_sel_hi:[0,1,1] neg_lo:[1,0,0] neg_hi:[1,0,0]\n\t" \
  "s_waitcnt lgkmcnt(0)\n\t" \
  "v_pk_fma_f32 v[64:65], v[240:241], v[144:145], v[64:65] op_sel_hi:[0,1,1] neg_lo:[1,0,0] neg_hi:[1,0,0]\n\t" \
  "v_pk_fma_f32 v[66:67], v[240:241], v[146:147], v[66:67] op_sel_hi:[0,1,1] neg_lo:[1,0,0] neg_hi:[1,0,0]\n\t" \
  "v_pk_fma_f32 v[68:69], v[240:241], v[148:149], v[68:69] op_sel_hi:[0,1,1] neg_lo:[1,0,0] neg_hi:[1,0,0]\n\t" \
  "v_pk_fma_f32 v[70:71], v[240:241], v[150:151], v[70:71] op_sel_hi:[0,1,1] neg_lo:[1,0,0] neg_hi:[1,0,0]\n\t" \
  "s_cmp_eq_u32 s42, 0\n\t" \
  "s_cbranch_scc1 .Lsk1%=\n\t" \
  "s_waitcnt vmcnt(4)\n\t" \
  "s_mov_b32 exec_hi, 0xfffffff\n\t" \
  "ds_write_b128 v218, v[160:163] offset:0\n\t" \
  "ds_write_b128 v218, v[164:167] offset:1008\n\t" \
  "ds_write_b128 v218, v[168:171] offset:1680\n\t" \
  "s_mov_b64 exec, 1\n\t" \
  "ds_add_u32 v245, v247\n\t" \
  "s_mov_b64 exec, -1\n\t" \
  ".Lsk1%=:\n\t" \
  "s_bitcmp1_b32 s32, 1\n\t" \
  "s_cbranch_scc1 .Lr1a%=\n\t" \
  "v_mfma_f32_32x32x8_bf16 v[192:207], v[212:213], v[212:213], 0\n\t" \
  "v_max_f32 v0, 0, v0\n\t" \
  "v_max_f32 v1, 0, v1\n\t" \
  "v_max_f32 v2, 0, v2\n\t" \
  "v_max_f32 v3, 0, v3\n\t" \
  "v_max_f32 v4, 0, v4\n\t" \
  "v_max_f32 v5, 0, v5\n\t" \
  "v_max_f32 v6, 0, v6\n\t" \
  "v_max_f32 v7, 0, v7\n\t" \
  "v_max_f32 v8, 0, v8\n\t" \
  "v_max_f32 v9, 0, v9\n\t" \
  "v_max_f32 v10, 0, v10\n\t" \
  "v_max_f32 v11, 0, v11\n\t" \
  "v_max_f32 v12, 0, v12\n\t" \
  "v_max_f32 v13, 0, v13\n\t" \
  "v_max_f32 v14, 0, v14\n\t" \
  "v_max_f32 v15, 0, v15\n\t" \
  "v_max_f32 v16, 0, v16\n\t" \
  "v_max_f32 v17, 0, v17\n\t" \
  "v_max_f32 v18, 0, v18\n\t" \
  "v_max_f32 v19, 0, v19\n\t" \
  "v_max_f32 v20, 0, v20\n\t" \
  "v_max_f32 v21, 0, v21\n\t" \
  "v_max_f32 v22, 0, v22\n\t" \
  "v_max_f32 v23, 0, v23\n\t" \
  "v_max_f32 v24, 0, v24\n\t" \
  "v_max_f32 v25, 0, v25\n\t" \
  "v_max_f32 v26, 0, v26\n\t" \
  "v_max_f32 v27, 0, v27\n\t" \
  "v_max_f32 v28, 0, v28\n\t" \
  "v_max_f32 v29, 0, v29\n\t" \
  "v_max_f32 v30, 0, v30\n\t" \
  "v_max_f32 v31, 0, v31\n\t" \
  "s_branch .Lr2a%=\n\t" \
  ".Lr1a%=:\n\t" \
  "v_mfma_f32_32x32x8_bf16 v[192:207], v[212:213], v[212:213], 0\n\t" \
  "v_max_f32 v32, 0, v32\n\t" \
  "v_max_f32 v33, 0, v33\n\t" \
  "v_max_f32 v34, 0, v34\n\t" \
  "v_max_f32 v35, 0, v35\n\t" \
  "v_max_f32 v36, 0, v36\n\t" \
  "v_max_f32 v37, 0, v37\n\t" \
  "v_max_f32 v38, 0, v38\n\t" \
  "v_max_f32 v39, 0, v39\n\t" \
  "v_max_f32 v40, 0, v40\n\t" \
  "v_max_f32 v41, 0, v41\n\t" \
  "v_max_f32 v42, 0, v42\n\t" \
  "v_max_f32 v43, 0, v43\n\t" \
  "v_max_f32 v44, 0, v44\n\t" \
  "v_max_f32 v45, 0, v45\n\t" \
  "v_max_f32 v46, 0, v46\n\t" \
  "v_max_f32 v47, 0, v47\n\t" \
  "v_max_f32 v48, 0, v48\n\t" \
  "v_max_f32 v49, 0, v49\n\t" \
  "v_max_f32 v50, 0, v50\n\t" \
  "v_max_f32 v51, 0, v51\n\t" \
  "v_max_f32 v52, 0, v52\n\t" \
  "v_max_f32 v53, 0, v53\n\t" \
  "v_max_f32 v54, 0, v54\n\t" \
  "v_max_f32 v55, 0, v55\n\t" \
  "v_max_f32 v56, 0, v56\n\t" \
  "v_max_f32 v57, 0, v57\n\t" \
  "v_max_f32 v58, 0, v58\n\t" \
  "v_max_f32 v59, 0, v59\n\t" \
  "v_max_f32 v60, 0, v60\n\t" \
  "v_max_f32 v61, 0, v61\n\t" \
  "v_max_f32 v62, 0, v62\n\t" \
  "v_max_f32 v63, 0, v63\n\t" \
  ".Lr2a%=:\n\t" \
  "v_pk_mul_f32 v[0:1], v[0:1], v[32:33]\n\t" \
  "v_pk_mul_f32 v[2:3], v[2:3], v[34:35]\n\t" \
  "v_pk_mul_f32 v[4:5], v[4:5], v[36:37]\n\t" \
  "v_pk_mul_f32 v[6:7], v[6:7], v[38:39]\n\t" \
  "v_pk_mul_f32 v[8:9], v[8:9], v[40:41]\n\t" \
  "v_pk_mul_f32 v[10:11], v[10:11], v[42:43]\n\t" \
  "v_pk_mul_f32 v[12:13], v[12:13], v[44:45]\n\t" \
  "v_pk_mul_f32 v[14:15], v[14:15], v[46:47]\n\t" \
  "v_pk_mul_f32 v[16:17], v[16:17], v[48:49]\n\t" \
  "v_pk_mul_f32 v[18:19], v[18:19], v[50:51]\n\t" \
  "v_pk_mul_f32 v[20:21], v[20:21], v[52:53]\n\t" \
  "v_pk_mul_f32 v[22:23], v[22:23], v[54:55]\n\t" \
  "v_pk_mul_f32 v[24:25], v[24:25], v[56:57]\n\t" \
  "v_pk_mul_f32 v[26:27], v[26:27], v[58:59]\n\t" \
  "v_pk_mul_f32 v[28:29], v[28:29], v[60:61]\n\t" \
  "v_pk_mul_f32 v[30:31], v[30:31], v[62:63]\n\t" \
  "v_pk_mul_f32 v[0:1], v[242:243], v[0:1] op_sel_hi:[0,1]\n\t" \
  "v_pk_mul_f32 v[2:3], v[242:243], v[2:3] op_sel_hi:[0,1]\n\t" \
  "v_pk_mul_f32 v[4:5], v[246:247], v[4:5] op_sel_hi:[0,1]\n\t" \
  "v_pk_mul_f32 v[6:7], v[246:247], v[6:7] op_sel_hi:[0,1]\n\t" \
  "v_pk_mul_f32 v[8:9], v[250:251], v[8:9] op_sel_hi:[0,1]\n\t" \
  "v_pk_mul_f32 v[10:11], v[250:251], v[10:11] op_sel_hi:[0,1]\n\t" \
  "v_pk_mul_f32 v[12:13], v[254:255], v[12:13] op_sel_hi:[0,1]\n\t" \
  "v_pk_mul_f32 v[14:15], v[254:255], v[14:15] op_sel_hi:[0,1]\n\t" \
  "v_pk_mul_f32 v[16:17], v[242:243], v[16:17] op_sel_hi:[0,1]\n\t" \
  "v_pk_mul_f32 v[18:19], v[242:243], v[18:19] op_sel_hi:[0,1]\n\t" \
  "v_pk_mul_f32 v[20:21], v[246:247], v[20:21] op_sel_hi:[0,1]\n\t" \
  "v_pk_mul_f32 v[22:23], v[246:247], v[22:23] op_sel_hi:[0,1]\n\t" \
  "v_pk_mul_f32 v[24:25], v[250:251], v[24:25] op_sel_hi:[0,1]\n\t" \
  "v_pk_mul_f32 v[26:27], v[250:251], v[26:27] op_sel_hi:[0,1]\n\t" \
  "v_pk_mul_f32 v[28:29], v[254:255], v[28:29] op_sel_hi:[0,1]\n\t" \
  "v_pk_mul_f32 v[30:31], v[254:255], v[30:31] op_sel_hi:[0,1]\n\t" \
  "s_cmp_eq_u32 s42, 0\n\t" \
  "s_cbranch_scc1 .Lsk2%=\n\t" \
  ".Laqse%=:\n\t" \
  "ds_read_b32 v219, v245\n\t" \
  "s_waitcnt lgkmcnt(0)\n\t" \
  "v_cmp_gt_u32 vcc, 4, v219\n\t" \
  "s_nop 1\n\t" \
  "s_cbranch_vccnz .Laqse%=\n\t" \
  "s_mov_b32 exec_hi, 0\n\t" \
  "ds_read_b128 v[192:195], v239 offset:256\n\t" \
  "ds_read_b128 v[196:199], v239 offset:272\n\t" \
  "ds_read_b128 v[200:203], v239 offset:288\n\t" \
  "ds_read_b128 v[204:207], v239 offset:304\n\t" \
  "s_mov_b32 exec_hi, -1\n\t" \
  "ds_read_b64 v[160:161], v239 offset:0\n\t" \
  "ds_read_b64 v[162:163], v239 offset:64\n\t" \
  "ds_read_b64 v[164:165], v239 offset:8\n\t" \
  "ds_read_b64 v[166:167], v239 offset:72\n\t" \
  "ds_read_b64 v[168:169], v239 offset:16\n\t" \
  "ds_read_b64 v[170:171], v239 offset:80\n\t" \
  "ds_read_b64 v[172:173], v239 offset:24\n\t" \
  "ds_read_b64 v[174:175], v239 offset:88\n\t" \
  "ds_read_b64 v[176:177], v239 offset:32\n\t" \
  "ds_read_b64 v[178:179], v239 offset:96\n\t" \
  "ds_read_b64 v[180:181], v239 offset:40\n\t" \
  "ds_read_b64 v[182:183], v239 offset:104\n\t" \
  "ds_read_b64 v[184:185], v239 offset:48\n\t" \
  "ds_read_b64 v[186:187], v239 offset:112\n\t" \
  "ds_read_b64 v[188:189], v239 offset:56\n\t" \
  "ds_read_b64 v[190:191], v239 offset:120\n\t" \
  "s_mov_b64 exec, 1\n\t" \
  "ds_add_u32 v245, v247 offset:8\n\t" \
  "s_mov_b64 exec, -1\n\t" \
  ".Lsk2%=:\n\t" \
  "s_bitcmp1_b32 s32, 0\n\t" \
  "s_cbranch_scc1 .Lr5a%=\n\t" \
  "v_cvt_pk_bf16_f32 v96, v0, v1\n\t" \
  "v_cvt_pk_bf16_f32 v97, v2, v3\n\t" \
  "v_cvt_pk_bf16_f32 v98, v16, v17\n\t" \
  "v_cvt_pk_bf16_f32 v99, v18, v19\n\t" \
  "v_cvt_pk_bf16_f32 v100, v4, v5\n\t" \
  "v_cvt_pk_bf16_f32 v101, v6, v7\n\t" \
  "v_cvt_pk_bf16_f32 v102, v20, v21\n\t" \
  "v_cvt_pk_bf16_f32 v103, v22, v23\n\t" \
  "v_cvt_pk_bf16_f32 v104, v8, v9\n\t" \
  "v_cvt_pk_bf16_f32 v105, v10, v11\n\t" \
  "v_cvt_pk_bf16_f32 v106, v24, v25\n\t" \
  "v_cvt_pk_bf16_f32 v107, v26, v27\n\t" \
  "v_cvt_pk_bf16_f32 v108, v12, v13\n\t" \
  "v_cvt_pk_bf16_f32 v109, v14, v15\n\t" \
  "v_cvt_pk_bf16_f32 v110, v28, v29\n\t" \
  "v_cvt_pk_bf16_f32 v111, v30, v31\n\t" \
  "s_branch .Lr6a%=\n\t" \
  ".Lr5a%=:\n\t" \
  "v_cvt_pk_bf16_f32 v98, v0, v1\n\t" \
  "v_cvt_pk_bf16_f32 v99, v2, v3\n\t" \
  "v_cvt_pk_bf16_f32 v96, v16, v17\n\t" \
  "v_cvt_pk_bf16_f32 v97, v18, v19\n\t" \
  "v_cvt_pk_bf16_f32 v102, v4, v5\n\t" \
  "v_cvt_pk_bf16_f32 v103, v6, v7\n\t" \
  "v_cvt_pk_bf16_f32 v100, v20, v21\n\t" \
  "v_cvt_pk_bf16_f32 v101, v22, v23\n\t" \
  "v_cvt_pk_bf16_f32 v106, v8, v9\n\t" \
  "v_cvt_pk_bf16_f32 v107, v10, v11\n\t" \
  "v_cvt_pk_bf16_f32 v104, v24, v25\n\t" \
  "v_cvt_pk_bf16_f32 v105, v26, v27\n\t" \
  "v_cvt_pk_bf16_f32 v110, v12, v13\n\t" \
  "v_cvt_pk_bf16_f32 v111, v14, v15\n\t" \
  "v_cvt_pk_bf16_f32 v108, v28, v29\n\t" \
  "v_cvt_pk_bf16_f32 v109, v30, v31\n\t" \
  ".Lr6a%=:\n\t" \
  "v_max_f32 v64, 0, v64\n\t" \
  "v_max_f32 v65, 0, v65\n\t" \
  "v_max_f32 v66, 0, v66\n\t" \
  "v_max_f32 v67, 0, v67\n\t" \
  "v_pk_mul_f32 v[64:65], v[64:65], v[68:69]\n\t" \
  "v_pk_mul_f32 v[66:67], v[66:67], v[70:71]\n\t" \
  "v_pk_mul_f32 v[64:65], v[242:243], v[64:65] op_sel_hi:[0,1]\n\t" \
  "v_pk_mul_f32 v[66:67], v[242:243], v[66:67] op_sel_hi:[0,1]\n\t" \
  "v_cvt_pk_bf16_f32 v114, v64, v65\n\t" \
  "v_cvt_pk_bf16_f32 v115, v66, v67\n\t" \
  "s_cmp_eq_u32 s42, 1\n\t" \
  "s_cbranch_scc1 .Laset%=\n\t" \
  ".Lahead%=:\n\t" \
  "s_mov_b32 s63, 0x400000\n\t" \
  ".Lfaa%=:\n\t" \
  "global_load_dwordx2 v[234:235], v233, s[30:31] sc1\n\t" \
  "s_sub_u32 s63, s63, 1\n\t" \
  "s_waitcnt vmcnt(0)\n\t" \
  "v_cmp_ne_u64 vcc, s[28:29], v[234:235]\n\t" \
  "s_nop 1\n\t" \
  "s_cbranch_vccz .Lfab%=\n\t" \
  "s_sleep 2\n\t" \
  "s_cbranch_scc0 .Lfaa%=\n\t" \
  ".Lfab%=:\n\t" \
  "s_mov_b32 exec_hi, 0xfffffff\n\t" \
  "global_load_dwordx4 v[160:163], v237, s[38:39] offset:0 sc1\n\t" \
  "global_load_dwordx4 v[164:167], v237, s[38:39] offset:960 sc1\n\t" \
  "global_load_dwordx4 v[168:171], v237, s[38:39] offset:1600 sc1\n\t" \
  "s_mov_b32 exec_hi, -1\n\t" \
  "global_load_dwordx4 v[208:211], v237, s[36:37] offset:0 sc1\n\t" \
  "global_load_dwordx2 v[216:217], v238, s[36:37] offset:3072 sc1\n\t" \
  "global_load_dwordx4 v[220:223], v237, s[36:37] offset:1024 sc1\n\t" \
  "global_load_dwordx4 v[224:227], v237, s[36:37] offset:2048 sc1\n\t" \
  "s_waitcnt vmcnt(4)\n\t" \
  "s_mov_b32 exec_hi, 0xfffffff\n\t" \
  "ds_write_b128 v218, v[160:163] offset:0\n\t" \
  "ds_write_b128 v218, v[164:167] offset:1008\n\t" \
  "ds_write_b128 v218, v[168:171] offset:1680\n\t" \
  "s_mov_b64 exec, 1\n\t" \
  "ds_add_u32 v245, v247\n\t" \
  "s_mov_b64 exec, -1\n\t" \
  ".Laqsl%=:\n\t" \
  "ds_read_b32 v219, v245\n\t" \
  "s_waitcnt lgkmcnt(0)\n\t" \
  "v_cmp_gt_u32 vcc, 4, v219\n\t" \
  "s_nop 1\n\t" \
  "s_cbranch_vccnz .Laqsl%=\n\t" \
  "s_mov_b32 exec_hi, 0\n\t" \
  "ds_read_b128 v[192:195], v239 offset:256\n\t" \
  "ds_read_b128 v[196:199], v239 offset:272\n\t" \
  "ds_read_b128 v[200:203], v239 offset:288\n\t" \
  "ds_read_b128 v[204:207], v239 offset:304\n\t" \
  "s_mov_b32 exec_hi, -1\n\t" \
  "ds_read_b64 v[160:161], v239 offset:0\n\t" \
  "ds_read_b64 v[162:163], v239 offset:64\n\t" \
  "ds_read_b64 v[164:165], v239 offset:8\n\t" \
  "ds_read_b64 v[166:167], v239 offset:72\n\t" \
  "ds_read_b64 v[168:169], v239 offset:16\n\t" \
  "ds_read_b64 v[170:171], v239 offset:80\n\t" \
  "ds_read_b64 v[172:173], v239 offset:24\n\t" \
  "ds_read_b64 v[174:175], v239 offset:88\n\t" \
  "ds_read_b64 v[176:177], v239 offset:32\n\t" \
  "ds_read_b64 v[178:179], v239 offset:96\n\t" \
  "ds_read_b64 v[180:181], v239 offset:40\n\t" \
  "ds_read_b64 v[182:183], v239 offset:104\n\t" \
  "ds_read_b64 v[184:185], v239 offset:48\n\t" \
  "ds_read_b64 v[186:187], v239 offset:112\n\t" \
  "ds_read_b64 v[188:189], v239 offset:56\n\t" \
  "ds_read_b64 v[190:191], v239 offset:120\n\t" \
  "s_mov_b64 exec, 1\n\t" \
  "ds_add_u32 v245, v247 offset:8\n\t" \
  "s_mov_b64 exec, -1\n\t" \
  ".Laset%=:\n\t" \
  "s_mov_b32 s42, 0\n\t" \
  "s_mov_b64 s[50:51], -1\n\t" \
  "s_mov_b64 s[52:53], -1\n\t" \
  "s_mov_b32 s43, 0x3c000000\n\t" \
  ".Lago%=:\n\t" \
  "s_add_u32 s40, s36, 7168\n\t" \
  "s_addc_u32 s41, s37, 0\n\t" \
  "v_mov_b64 v[254:255], 0\n\t" \
  "s_lshl_b32 s63, s32, 2\n\t" \
  "v_mov_b32 v252, s63\n\t" \
  "global_load_dwordx4 v[212:215], v237, s[40:41] offset:-3584 sc1\n\t" \
  "global_load_dwordx2 v[218:219], v238, s[40:41] offset:-512 sc1\n\t" \
  "global_load_dwordx4 v[228:231], v237, s[40:41] offset:-2560 sc1\n\t" \
  "global_load_dwordx4 v[232:235], v237, s[40:41] offset:-1536 sc1\n\t" \
  "ds_write_b32 v252, v236 offset:768\n\t" \
  "v_mfma_f32_32x32x8_bf16 v[0:15], v[254:255], v[254:255], 0\n\t" \
  "s_mul_i32 s63, s32, 5120\n\t" \
  "s_add_u32 s63, s63, 1024\n\t" \
  "v_add_u32 v250, s63, v237\n\t" \
  "s_cmp_eq_u32 s32, 0\n\t" \
  "s_cbranch_scc0 .Lan5%=\n\t" \
  "v_lshrrev_b32 v132, 4, v237\n\t" \
  "v_and_b32 v129, 3, v132\n\t" \
  "v_mul_u32_u24 v128, 10304, v129\n\t" \
  "v_add_u32 v128, 62720, v128\n\t" \
  "v_mul_u32_u24 v129, 2080, v129\n\t" \
  "v_add_u32 v129, 112256, v129\n\t" \
  "v_bfe_u32 v130, v132, 2, 2\n\t" \
  "v_lshrrev_b32 v131, 4, v132\n\t" \
  "v_lshl_or_b32 v130, v131, 3, v130\n\t" \
  "v_add_u32 v131, 128, v130\n\t" \
  "v_lshl_add_u32 v244, v131, 4, v128\n\t" \
  "v_mul_u32_u24 v131, 205, v131\n\t" \
  "v_lshrrev_b32 v246, 10, v131\n\t" \
  "v_add_u32 v131, 820, v131\n\t" \
  "v_lshrrev_b32 v248, 10, v131\n\t" \
  "v_lshl_add_u32 v246, v246, 4, v129\n\t" \
  "v_lshl_add_u32 v248, v248, 4, v129\n\t" \
  ".Lan5%=:\n\t" \
  "s_mov_b32 s63, 6\n\t" \
  "s_waitcnt vmcnt(4) lgkmcnt(0)\n\t" \
  "v_mfma_f32_32x32x16_bf16 v[128:143], v[208:211], v[160:163], 0\n\t" \
  "v_mfma_f32_32x32x8_bf16 v[128:143], v[216:217], v[192:193], v[128:143]\n\t" \
  "s_setprio 1\n\t" \
  "v_add_u32 v240, 64, v241\n\t" \
  "v_mov_b32 v242, 10304\n\t" \
  "s_lshl_b32 s64, s32, 9\n\t" \
  "s_add_u32 s64, s64, 103424\n\t" \
  "s_mov_b32 exec_lo, 0\n\t" \
  "v_add_u32 v240, s64, v237\n\t" \
  "v_mov_b32 v242, 2080\n\t" \
  "s_mov_b32 exec_lo, -1\n\t" \
  "s_nop 2\n\t" \
  "v_exp_f32 v128, v128\n\t" \
  "v_exp_f32 v129, v129\n\t" \
  "v_mfma_f32_32x32x8_bf16 v[16:31], v[254:255], v[254:255], 0\n\t" \
  "ds_write_b128 v250, v[96:99] offset:0\n\t" \
  "v_exp_f32 v130, v130\n\t" \
  "v_exp_f32 v131, v131\n\t" \
  "v_exp_f32 v132, v132\n\t" \
  "v_mfma_f32_32x32x16_bf16 v[144:159], v[208:211], v[164:167], 0\n\t" \
  "v_exp_f32 v133, v133\n\t" \
  "v_exp_f32 v134, v134\n\t" \
  "v_exp_f32 v135, v135\n\t" \
  "v_mfma_f32_32x32x8_bf16 v[144:159], v[216:217], v[194:195], v[144:159]\n\t" \
  "v_exp_f32 v136, v136\n\t" \
  "v_exp_f32 v137, v137\n\t" \
  "v_exp_f32 v138, v138\n\t" \
  "v_mfma_f32_32x32x8_bf16 v[32:47], v[254:255], v[254:255], 0\n\t" \
  "v_exp_f32 v139, v139\n\t" \
  "v_exp_f32 v140, v140\n\t" \
  "v_exp_f32 v141, v141\n\t" \
  "v_smfmac_f32_32x32x32_bf16 v[0:15], v[220:223], v[128:135], v236\n\t" \
  "ds_write_b128 v250, v[100:103] offset:1024\n\t" \
  "v_exp_f32 v142, v142\n\t" \
  "v_exp_f32 v143, v143\n\t" \
  "v_exp_f32 v144, v144\n\t" \
  "v_exp_f32 v145, v145\n\t" \
  "v_smfmac_f32_32x32x32_bf16 v[0:15], v[224:227], v[136:143], v236\n\t" \
  "ds_write_b128 v250, v[104:107] offset:2048\n\t" \
  "v_exp_f32 v146, v146\n\t" \
  "v_exp_f32 v147, v147\n\t" \
  "v_exp_f32 v148, v148\n\t" \
  "v_mfma_f32_32x32x16_bf16 v[128:143], v[208:211], v[168:171], 0\n\t" \
  "v_exp_f32 v149, v149\n\t" \
  "v_exp_f32 v150, v150\n\t" \
  "v_exp_f32 v151, v151\n\t" \
  "v_mfma_f32_32x32x8_bf16 v[128:143], v[216:217], v[196:197], v[128:143]\n\t" \
  "v_exp_f32 v152, v152\n\t" \
  "v_exp_f32 v153, v153\n\t" \
  "v_exp_f32 v154, v154\n\t" \
  "v_mfma_f32_32x32x8_bf16 v[48:63], v[254:255], v[254:255], 0\n\t" \
  "v_exp_f32 v155, v155\n\t" \
  "v_exp_f32 v156, v156\n\t" \
  "v_exp_f32 v157, v157\n\t" \
  "v_smfmac_f32_32x32x32_bf16 v[16:31], v[220:223], v[144:151], v236\n\t" \
  "ds_write_b128 v250, v[108:111] offset:3072\n\t" \
  "v_exp_f32 v158, v158\n\t" \
  "v_exp_f32 v159, v159\n\t" \
  "v_exp_f32 v128, v128\n\t" \
  "v_exp_f32 v129, v129\n\t" \
  "v_smfmac_f32_32x32x32_bf16 v[16:31], v[224:227], v[152:159], v236\n\t" \
  "ds_write_b64 v250, v[114:115] offset:4096\n\t" \
  "v_exp_f32 v130, v130\n\t" \
  "v_exp_f32 v131, v131\n\t" \
  "v_exp_f32 v132, v132\n\t" \
  "v_mfma_f32_32x32x16_bf16 v[144:159], v[208:211], v[172:175], 0\n\t" \
  "v_exp_f32 v133, v133\n\t" \
  "v_exp_f32 v134, v134\n\t" \
  "v_exp_f32 v135, v135\n\t" \
  "v_mfma_f32_32x32x8_bf16 v[144:159], v[216:217], v[198:199], v[144:159]\n\t" \
  "v_exp_f32 v136, v136\n\t" \
  "v_exp_f32 v137, v137\n\t" \
  "v_exp_f32 v138, v138\n\t" \
  "v_mfma_f32_32x32x8_bf16 v[64:79], v[254:255], v[254:255], 0\n\t" \
  "v_exp_f32 v139, v139\n\t" \
  "v_exp_f32 v140, v140\n\t" \
  "v_exp_f32 v141, v141\n\t" \
  "v_smfmac_f32_32x32x32_bf16 v[32:47], v[220:223], v[128:135], v236\n\t" \
  "ds_write_b32 v252, v236 offset:784\n\t" \
  "v_exp_f32 v142, v142\n\t" \
  "v_exp_f32 v143, v143\n\t" \
  "v_exp_f32 v144, v144\n\t" \
  "v_exp_f32 v145, v145\n\t" \
  "v_smfmac_f32_32x32x32_bf16 v[32:47], v[224:227], v[136:143], v236\n\t" \
  "v_exp_f32 v146, v146\n\t" \
  "v_exp_f32 v147, v147\n\t" \
  "v_exp_f32 v148, v148\n\t" \
  "v_mfma_f32_32x32x16_bf16 v[128:143], v[208:211], v[176:179], 0\n\t" \
  "v_exp_f32 v149, v149\n\t" \
  "v_exp_f32 v150, v150\n\t" \
  "v_exp_f32 v151, v151\n\t" \
  "v_mfma_f32_32x32x8_bf16 v[128:143], v[216:217], v[200:201], v[128:143]\n\t" \
  "v_exp_f32 v152, v152\n\t" \
  "v_exp_f32 v153, v153\n\t" \
  "v_exp_f32 v154, v154\n\t" \
  "v_mfma_f32_32x32x8_bf16 v[80:95], v[254:255], v[254:255], 0\n\t" \
  "v_exp_f32 v155, v155\n\t" \
  "v_exp_f32 v156, v156\n\t" \
  "v_exp_f32 v157, v157\n\t" \
  "v_smfmac_f32_32x32x32_bf16 v[48:63], v[220:223], v[144:151], v236\n\t" \
  "v_exp_f32 v158, v158\n\t" \
  "v_exp_f32 v159, v159\n\t" \
  "v_exp_f32 v128, v128\n\t" \
  "v_exp_f32 v129, v129\n\t" \
  "v_smfmac_f32_32x32x32_bf16 v[48:63], v[224:227], v[152:159], v236\n\t" \
  "v_exp_f32 v130, v130\n\t" \
  "v_exp_f32 v131, v131\n\t" \
  "v_exp_f32 v132, v132\n\t" \
  "v_mfma_f32_32x32x16_bf16 v[144:159], v[208:211], v[180:183], 0\n\t" \
  "v_exp_f32 v133, v133\n\t" \
  "v_exp_f32 v134, v134\n\t" \
  "v_exp_f32 v135, v135\n\t" \
  "v_mfma_f32_32x32x8_bf16 v[144:159], v[216:217], v[202:203], v[144:159]\n\t" \
  "v_exp_f32 v136, v136\n\t" \
  "v_exp_f32 v137, v137\n\t" \
  "v_exp_f32 v138, v138\n\t" \
  "v_mfma_f32_32x32x8_bf16 v[96:111], v[254:255], v[254:255], 0\n\t" \
  "v_exp_f32 v139, v139\n\t" \
  "v_exp_f32 v140, v140\n\t" \
  "v_exp_f32 v141, v141\n\t" \
  "v_smfmac_f32_32x32x32_bf16 v[64:79], v[220:223], v[128:135], v236\n\t" \
  "v_exp_f32 v142, v142\n\t" \
  "v_exp_f32 v143, v143\n\t" \
  "v_exp_f32 v144, v144\n\t" \
  "v_exp_f32 v145, v145\n\t" \
  "v_smfmac_f32_32x32x32_bf16 v[64:79], v[224:227], v[136:143], v236\n\t" \
  "v_exp_f32 v146, v146\n\t" \
  "v_exp_f32 v147, v147\n\t" \
  "v_exp_f32 v148, v148\n\t" \
  "v_mfma_f32_32x32x16_bf16 v[128:143], v[208:211], v[184:187], 0\n\t" \
  "v_exp_f32 v149, v149\n\t" \
  "v_exp_f32 v150, v150\n\t" \
  "v_exp_f32 v151, v151\n\t" \
  "v_mfma_f32_32x32x8_bf16 v[128:143], v[216:217], v[204:205], v[128:143]\n\t" \
  "v_exp_f32 v152, v152\n\t" \
  "v_exp_f32 v153, v153\n\t" \
  "v_exp_f32 v154, v154\n\t" \
  "v_mfma_f32_32x32x8_bf16 v[112:127], v[254:255], v[254:255], 0\n\t" \
  "v_exp_f32 v155, v155\n\t" \
  "v_exp_f32 v156, v156\n\t" \
  "v_exp_f32 v157, v157\n\t" \
  "v_smfmac_f32_32x32x32_bf16 v[80:95], v[220:223], v[144:151], v236\n\t" \
  "v_exp_f32 v158, v158\n\t" \
  "v_exp_f32 v159, v159\n\t" \
  "v_exp_f32 v128, v128\n\t" \
  "v_exp_f32 v129, v129\n\t" \
  "v_smfmac_f32_32x32x32_bf16 v[80:95], v[224:227], v[152:159], v236\n\t" \
  "v_exp_f32 v130, v130\n\t" \
  "v_exp_f32 v131, v131\n\t" \
  "v_exp_f32 v132, v132\n\t" \
  "v_exp_f32 v133, v133\n\t" \
  "v_mfma_f32_32x32x16_bf16 v[144:159], v[208:211], v[188:191], 0\n\t" \
  "v_exp_f32 v134, v134\n\t" \
  "v_exp_f32 v135, v135\n\t" \
  "v_exp_f32 v136, v136\n\t" \
  "v_exp_f32 v137, v137\n\t" \
  "v_mfma_f32_32x32x8_bf16 v[144:159], v[216:217], v[206:207], v[144:159]\n\t" \
  "v_exp_f32 v138, v138\n\t" \
  "v_exp_f32 v139, v139\n\t" \
  "v_exp_f32 v140, v140\n\t" \
  "v_exp_f32 v141, v141\n\t" \
  "v_smfmac_f32_32x32x32_bf16 v[96:111], v[220:223], v[128:135], v236\n\t" \
  "global_load_dwordx4 v[208:211], v237, s[40:41] offset:0 sc1\n\t" \
  "global_load_dwordx2 v[216:217], v238, s[40:41] offset:3072 sc1\n\t" \
  "v_exp_f32 v142, v142\n\t" \
  "v_exp_f32 v143, v143\n\t" \
  "v_exp_f32 v144, v144\n\t" \
  "v_exp_f32 v145, v145\n\t" \
  "v_smfmac_f32_32x32x32_bf16 v[96:111], v[224:227], v[136:143], v236\n\t" \
  "s_waitcnt vmcnt(4) lgkmcnt(0)\n\t" \
  "v_exp_f32 v146, v146\n\t" \
  "v_exp_f32 v147, v147\n\t" \
  "v_exp_f32 v148, v148\n\t" \
  "v_exp_f32 v149, v149\n\t" \
  "v_mfma_f32_32x32x16_bf16 v[128:143], v[212:215], v[160:163], 0\n\t" \
  "v_exp_f32 v150, v150\n\t" \
  "v_exp_f32 v151, v151\n\t" \
  "v_exp_f32 v152, v152\n\t" \
  "v_exp_f32 v153, v153\n\t" \
  "v_mfma_f32_32x32x8_bf16 v[128:143], v[218:219], v[192:193], v[128:143]\n\t" \
  "v_exp_f32 v154, v154\n\t" \
  "v_exp_f32 v155, v155\n\t" \
  "v_exp_f32 v156, v156\n\t" \
  "v_exp_f32 v157, v157\n\t" \
  "v_smfmac_f32_32x32x32_bf16 v[112:127], v[220:223], v[144:151], v236\n\t" \
  "global_load_dwordx4 v[220:223], v237, s[40:41] offset:1024 sc1\n\t" \
  "s_branch .Lattn2%=\n\t" \
  ".Lattn%=:\n\t" \
  "v_exp_f32 v158, v158\n\t" \
  "v_exp_f32 v159, v159\n\t" \
  "v_exp_f32 v128, v128\n\t" \
  "v_exp_f32 v129, v129\n\t" \
  "v_smfmac_f32_32x32x32_bf16 v[112:127], v[232:235], v[152:159], v236\n\t" \
  "global_load_dwordx4 v[232:235], v237, s[40:41] offset:-1536 sc1\n\t" \
  "s_waitcnt vmcnt(4)\n\t" \
  "v_exp_f32 v130, v130\n\t" \
  "v_exp_f32 v131, v131\n\t" \
  "v_exp_f32 v132, v132\n\t" \
  "v_exp_f32 v133, v133\n\t" \
  "v_mfma_f32_32x32x16_bf16 v[144:159], v[208:211], v[164:167], 0\n\t" \
  "v_exp_f32 v134, v134\n\t" \
  "v_exp_f32 v135, v135\n\t" \
  "v_exp_f32 v136, v136\n\t" \
  "v_exp_f32 v137, v137\n\t" \
  "v_mfma_f32_32x32x8_bf16 v[144:159], v[216:217], v[194:195], v[144:159]\n\t" \
  "v_exp_f32 v138, v138\n\t" \
  "v_exp_f32 v139, v139\n\t" \
  "v_exp_f32 v140, v140\n\t" \
  "v_exp_f32 v141, v141\n\t" \
  "v_smfmac_f32_32x32x32_bf16 v[0:15], v[220:223], v[128:135], v236\n\t" \
  "v_exp_f32 v142, v142\n\t" \
  "v_exp_f32 v143, v143\n\t" \
  "v_exp_f32 v144, v144\n\t" \
  "v_exp_f32 v145, v145\n\t" \
  "v_smfmac_f32_32x32x32_bf16 v[0:15], v[224:227], v[136:143], v236\n\t" \
  "v_exp_f32 v146, v146\n\t" \
  "v_exp_f32 v147, v147\n\t" \
  "v_exp_f32 v148, v148\n\t" \
  "v_exp_f32 v149, v149\n\t" \
  "v_mfma_f32_32x32x16_bf16 v[128:143], v[208:211], v[168:171], 0\n\t" \
  "v_exp_f32 v150, v150\n\t" \
  "v_exp_f32 v151, v151\n\t" \
  "v_exp_f32 v152, v152\n\t" \
  "v_exp_f32 v153, v153\n\t" \
  "v_mfma_f32_32x32x8_bf16 v[128:143], v[216:217], v[196:197], v[128:143]\n\t" \
  "v_exp_f32 v154, v154\n\t" \
  "v_exp_f32 v155, v155\n\t" \
  "v_exp_f32 v156, v156\n\t" \
  "v_exp_f32 v157, v157\n\t" \
  "v_smfmac_f32_32x32x32_bf16 v[16:31], v[220:223], v[144:151], v236\n\t" \
  "v_exp_f32 v158, v158\n\t" \
  "v_exp_f32 v159, v159\n\t" \
  "v_exp_f32 v128, v128\n\t" \
  "v_exp_f32 v129, v129\n\t" \
  "v_smfmac_f32_32x32x32_bf16 v[16:31], v[224:227], v[152:159], v236\n\t" \
  "v_exp_f32 v130, v130\n\t" \
  "v_exp_f32 v131, v131\n\t" \
  "v_exp_f32 v132, v132\n\t" \
  "v_exp_f32 v133, v133\n\t" \
  "v_mfma_f32_32x32x16_bf16 v[144:159], v[208:211], v[172:175], 0\n\t" \
  "v_exp_f32 v134, v134\n\t" \
  "v_exp_f32 v135, v135\n\t" \
  "v_exp_f32 v136, v136\n\t" \
  "v_exp_f32 v137, v137\n\t" \
  "v_mfma_f32_32x32x8_bf16 v[144:159], v[216:217], v[198:199], v[144:159]\n\t" \
  "v_exp_f32 v138, v138\n\t" \
  "v_exp_f32 v139, v139\n\t" \
  "v_exp_f32 v140, v140\n\t" \
  "v_exp_f32 v141, v141\n\t" \
  "v_smfmac_f32_32x32x32_bf16 v[32:47], v[220:223], v[128:135], v236\n\t" \
  "v_exp_f32 v142, v142\n\t" \
  "v_exp_f32 v143, v143\n\t" \
  "v_exp_f32 v144, v144\n\t" \
  "v_exp_f32 v145, v145\n\t" \
  "v_smfmac_f32_32x32x32_bf16 v[32:47], v[224:227], v[136:143], v236\n\t" \
  "v_exp_f32 v146, v146\n\t" \
  "v_exp_f32 v147, v147\n\t" \
  "v_exp_f32 v148, v148\n\t" \
  "v_exp_f32 v149, v149\n\t" \
  "v_mfma_f32_32x32x16_bf16 v[128:143], v[208:211], v[176:179], 0\n\t" \
  "v_exp_f32 v150, v150\n\t" \
  "v_exp_f32 v151, v151\n\t" \
  "v_exp_f32 v152, v152\n\t" \
  "v_exp_f32 v153, v153\n\t" \
  "v_mfma_f32_32x32x8_bf16 v[128:143], v[216:217], v[200:201], v[128:143]\n\t" \
  "v_exp_f32 v154, v154\n\t" \
  "v_exp_f32 v155, v155\n\t" \
  "v_exp_f32 v156, v156\n\t" \
  "v_exp_f32 v157, v157\n\t" \
  "v_smfmac_f32_32x32x32_bf16 v[48:63], v[220:223], v[144:151], v236\n\t" \
  "v_exp_f32 v158, v158\n\t" \
  "v_exp_f32 v159, v159\n\t" \
  "v_exp_f32 v128, v128\n\t" \
  "v_exp_f32 v129, v129\n\t" \
  "v_smfmac_f32_32x32x32_bf16 v[48:63], v[224:227], v[152:159], v236\n\t" \
  "v_exp_f32 v130, v130\n\t" \
  "v_exp_f32 v131, v131\n\t" \
  "v_exp_f32 v132, v132\n\t" \
  "v_exp_f32 v133, v133\n\t" \
  "v_mfma_f32_32x32x16_bf16 v[144:159], v[208:211], v[180:183], 0\n\t" \
  "v_exp_f32 v134, v134\n\t" \
  "v_exp_f32 v135, v135\n\t" \
  "v_exp_f32 v136, v136\n\t" \
  "v_exp_f32 v137, v137\n\t" \
  "v_mfma_f32_32x32x8_bf16 v[144:159], v[216:217], v[202:203], v[144:159]\n\t" \
  "v_exp_f32 v138, v138\n\t" \
  "v_exp_f32 v139, v139\n\t" \
  "v_exp_f32 v140, v140\n\t" \
  "v_exp_f32 v141, v141\n\t" \
  "v_smfmac_f32_32x32x32_bf16 v[64:79], v[220:223], v[128:135], v236\n\t" \
  "v_exp_f32 v142, v142\n\t" \
  "v_exp_f32 v143, v143\n\t" \
  "v_exp_f32 v144, v144\n\t" \
  "v_exp_f32 v145, v145\n\t" \
  "v_smfmac_f32_32x32x32_bf16 v[64:79], v[224:227], v[136:143], v236\n\t" \
  "v_exp_f32 v146, v146\n\t" \
  "v_exp_f32 v147, v147\n\t" \
  "v_exp_f32 v148, v148\n\t" \
  "v_exp_f32 v149, v149\n\t" \
  "v_mfma_f32_32x32x16_bf16 v[128:143], v[208:211], v[184:187], 0\n\t" \
  "v_exp_f32 v150, v150\n\t" \
  "v_exp_f32 v151, v151\n\t" \
  "v_exp_f32 v152, v152\n\t" \
  "v_exp_f32 v153, v153\n\t" \
  "v_mfma_f32_32x32x8_bf16 v[128:143], v[216:217], v[204:205], v[128:143]\n\t" \
  "v_exp_f32 v154, v154\n\t" \
  "v_exp_f32 v155, v155\n\t" \
  "v_exp_f32 v156, v156\n\t" \
  "v_exp_f32 v157, v157\n\t" \
  "v_smfmac_f32_32x32x32_bf16 v[80:95], v[220:223], v[144:151], v236\n\t" \
  "v_exp_f32 v158, v158\n\t" \
  "v_exp_f32 v159, v159\n\t" \
  "v_exp_f32 v128, v128\n\t" \
  "v_exp_f32 v129, v129\n\t" \
  "v_smfmac_f32_32x32x32_bf16 v[80:95], v[224:227], v[152:159], v236\n\t" \
  "v_exp_f32 v130, v130\n\t" \
  "v_exp_f32 v131, v131\n\t" \
  "v_exp_f32 v132, v132\n\t" \
  "v_exp_f32 v133, v133\n\t" \
  "v_mfma_f32_32x32x16_bf16 v[144:159], v[208:211], v[188:191], 0\n\t" \
  "v_exp_f32 v134, v134\n\t" \
  "v_exp_f32 v135, v135\n\t" \
  "v_exp_f32 v136, v136\n\t" \
  "v_exp_f32 v137, v137\n\t" \
  "v_mfma_f32_32x32x8_bf16 v[144:159], v[216:217], v[206:207], v[144:159]\n\t" \
  "v_exp_f32 v138, v138\n\t" \
  "v_exp_f32 v139, v139\n\t" \
  "v_exp_f32 v140, v140\n\t" \
  "v_exp_f32 v141, v141\n\t" \
  "v_smfmac_f32_32x32x32_bf16 v[96:111], v[220:223], v[128:135], v236\n\t" \
  "global_load_dwordx4 v[208:211], v237, s[40:41] offset:0 sc1\n\t" \
  "global_load_dwordx2 v[216:217], v238, s[40:41] offset:3072 sc1\n\t" \
  "v_exp_f32 v142, v142\n\t" \
  "v_exp_f32 v143, v143\n\t" \
  "v_exp_f32 v144, v144\n\t" \
  "v_exp_f32 v145, v145\n\t" \
  "v_smfmac_f32_32x32x32_bf16 v[96:111], v[224:227], v[136:143], v236\n\t" \
  "s_waitcnt vmcnt(4) lgkmcnt(0)\n\t" \
  "v_exp_f32 v146, v146\n\t" \
  "v_exp_f32 v147, v147\n\t" \
  "v_exp_f32 v148, v148\n\t" \
  "v_exp_f32 v149, v149\n\t" \
  "v_mfma_f32_32x32x16_bf16 v[128:143], v[212:215], v[160:163], 0\n\t" \
  "v_exp_f32 v150, v150\n\t" \
  "v_exp_f32 v151, v151\n\t" \
  "v_exp_f32 v152, v152\n\t" \
  "v_exp_f32 v153, v153\n\t" \
  "v_mfma_f32_32x32x8_bf16 v[128:143], v[218:219], v[192:193], v[128:143]\n\t" \
  "v_exp_f32 v154, v154\n\t" \
  "v_exp_f32 v155, v155\n\t" \
  "v_exp_f32 v156, v156\n\t" \
  "v_exp_f32 v157, v157\n\t" \
  "v_smfmac_f32_32x32x32_bf16 v[112:127], v[220:223], v[144:151], v236\n\t" \
  "global_load_dwordx4 v[220:223], v237, s[40:41] offset:1024 sc1\n\t" \
  ".Lattn2%=:\n\t" \
  "v_exp_f32 v158, v158\n\t" \
  "v_exp_f32 v159, v159\n\t" \
  "v_exp_f32 v128, v128\n\t" \
  "v_exp_f32 v129, v129\n\t" \
  "v_smfmac_f32_32x32x32_bf16 v[112:127], v[224:227], v[152:159], v236\n\t" \
  "global_load_dwordx4 v[224:227], v237, s[40:41] offset:2048 sc1\n\t" \
  "s_waitcnt vmcnt(4)\n\t" \
  "v_exp_f32 v130, v130\n\t" \
  "v_exp_f32 v131, v131\n\t" \
  "v_exp_f32 v132, v132\n\t" \
  "v_exp_f32 v133, v133\n\t" \
  "v_mfma_f32_32x32x16_bf16 v[144:159], v[212:215], v[164:167], 0\n\t" \
  "v_exp_f32 v134, v134\n\t" \
  "v_exp_f32 v135, v135\n\t" \
  "v_exp_f32 v136, v136\n\t" \
  "v_exp_f32 v137, v137\n\t" \
  "v_mfma_f32_32x32x8_bf16 v[144:159], v[218:219], v[194:195], v[144:159]\n\t" \
  "v_exp_f32 v138, v138\n\t" \
  "v_exp_f32 v139, v139\n\t" \
  "v_exp_f32 v140, v140\n\t" \
  "v_exp_f32 v141, v141\n\t" \
  "v_smfmac_f32_32x32x32_bf16 v[0:15], v[228:231], v[128:135], v236\n\t" \
  "v_exp_f32 v142, v142\n\t" \
  "v_exp_f32 v143, v143\n\t" \
  "v_exp_f32 v144, v144\n\t" \
  "v_exp_f32 v145, v145\n\t" \
  "v_smfmac_f32_32x32x32_bf16 v[0:15], v[232:235], v[136:143], v236\n\t" \
  "s_add_u32 s40, s40, 7168\n\t" \
  "s_addc_u32 s41, s41, 0\n\t" \
  "v_exp_f32 v146, v146\n\t" \
  "v_exp_f32 v147, v147\n\t" \
  "v_exp_f32 v148, v148\n\t" \
  "v_exp_f32 v149, v149\n\t" \
  "v_mfma_f32_32x32x16_bf16 v[128:143], v[212:215], v[168:171], 0\n\t" \
  "v_exp_f32 v150, v150\n\t" \
  "v_exp_f32 v151, v151\n\t" \
  "v_exp_f32 v152, v152\n\t" \
  "v_exp_f32 v153, v153\n\t" \
  "v_mfma_f32_32x32x8_bf16 v[128:143], v[218:219], v[196:197], v[128:143]\n\t" \
  "v_exp_f32 v154, v154\n\t" \
  "v_exp_f32 v155, v155\n\t" \
  "v_exp_f32 v156, v156\n\t" \
  "v_exp_f32 v157, v157\n\t" \
  "v_smfmac_f32_32x32x32_bf16 v[16:31], v[228:231], v[144:151], v236\n\t" \
  "v_exp_f32 v158, v158\n\t" \
  "v_exp_f32 v159, v159\n\t" \
  "v_exp_f32 v128, v128\n\t" \
  "v_exp_f32 v129, v129\n\t" \
  "v_smfmac_f32_32x32x32_bf16 v[16:31], v[232:235], v[152:159], v236\n\t" \
  "v_exp_f32 v130, v130\n\t" \
  "v_exp_f32 v131, v131\n\t" \
  "v_exp_f32 v132, v132\n\t" \
  "v_exp_f32 v133, v133\n\t" \
  "v_mfma_f32_32x32x16_bf16 v[144:159], v[212:215], v[172:175], 0\n\t" \
  "v_exp_f32 v134, v134\n\t" \
  "v_exp_f32 v135, v135\n\t" \
  "v_exp_f32 v136, v136\n\t" \
  "v_exp_f32 v137, v137\n\t" \
  "v_mfma_f32_32x32x8_bf16 v[144:159], v[218:219], v[198:199], v[144:159]\n\t" \
  "v_exp_f32 v138, v138\n\t" \
  "v_exp_f32 v139, v139\n\t" \
  "v_exp_f32 v140, v140\n\t" \
  "v_exp_f32 v141, v141\n\t" \
  "v_smfmac_f32_32x32x32_bf16 v[32:47], v[228:231], v[128:135], v236\n\t" \
  "v_exp_f32 v142, v142\n\t" \
  "v_exp_f32 v143, v143\n\t" \
  "v_exp_f32 v144, v144\n\t" \
  "v_exp_f32 v145, v145\n\t" \
  "v_smfmac_f32_32x32x32_bf16 v[32:47], v[232:235], v[136:143], v236\n\t" \
  "v_exp_f32 v146, v146\n\t" \
  "v_exp_f32 v147, v147\n\t" \
  "v_exp_f32 v148, v148\n\t" \
  "v_exp_f32 v149, v149\n\t" \
  "v_mfma_f32_32x32x16_bf16 v[128:143], v[212:215], v[176:179], 0\n\t" \
  "v_exp_f32 v150, v150\n\t" \
  "v_exp_f32 v151, v151\n\t" \
  "v_exp_f32 v152, v152\n\t" \
  "v_exp_f32 v153, v153\n\t" \
  "v_mfma_f32_32x32x8_bf16 v[128:143], v[218:219], v[200:201], v[128:143]\n\t" \
  "v_exp_f32 v154, v154\n\t" \
  "v_exp_f32 v155, v155\n\t" \
  "v_exp_f32 v156, v156\n\t" \
  "v_exp_f32 v157, v157\n\t" \
  "v_smfmac_f32_32x32x32_bf16 v[48:63], v[228:231], v[144:151], v236\n\t" \
  "v_exp_f32 v158, v158\n\t" \
  "v_exp_f32 v159, v159\n\t" \
  "v_exp_f32 v128, v128\n\t" \
  "v_exp_f32 v129, v129\n\t" \
  "v_smfmac_f32_32x32x32_bf16 v[48:63], v[232:235], v[152:159], v236\n\t" \
  "v_exp_f32 v130, v130\n\t" \
  "v_exp_f32 v131, v131\n\t" \
  "v_exp_f32 v132, v132\n\t" \
  "v_exp_f32 v133, v133\n\t" \
  "v_mfma_f32_32x32x16_bf16 v[144:159], v[212:215], v[180:183], 0\n\t" \
  "v_exp_f32 v134, v134\n\t" \
  "v_exp_f32 v135, v135\n\t" \
  "v_exp_f32 v136, v136\n\t" \
  "v_exp_f32 v137, v137\n\t" \
  "v_mfma_f32_32x32x8_bf16 v[144:159], v[218:219], v[202:203], v[144:159]\n\t" \
  "v_exp_f32 v138, v138\n\t" \
  "v_exp_f32 v139, v139\n\t" \
  "v_exp_f32 v140, v140\n\t" \
  "v_exp_f32 v141, v141\n\t" \
  "v_smfmac_f32_32x32x32_bf16 v[64:79], v[228:231], v[128:135], v236\n\t" \
  "v_exp_f32 v142, v142\n\t" \
  "v_exp_f32 v143, v143\n\t" \
  "v_exp_f32 v144, v144\n\t" \
  "v_exp_f32 v145, v145\n\t" \
  "v_smfmac_f32_32x32x32_bf16 v[64:79], v[232:235], v[136:143], v236\n\t" \
  "v_exp_f32 v146, v146\n\t" \
  "v_exp_f32 v147, v147\n\t" \
  "v_exp_f32 v148, v148\n\t" \
  "v_exp_f32 v149, v149\n\t" \
  "v_mfma_f32_32x32x16_bf16 v[128:143], v[212:215], v[184:187], 0\n\t" \
  "v_exp_f32 v150, v150\n\t" \
  "v_exp_f32 v151, v151\n\t" \
  "v_exp_f32 v152, v152\n\t" \
  "v_exp_f32 v153, v153\n\t" \
  "v_mfma_f32_32x32x8_bf16 v[128:143], v[218:219], v[204:205], v[128:143]\n\t" \
  "v_exp_f32 v154, v154\n\t" \
  "v_exp_f32 v155, v155\n\t" \
  "v_exp_f32 v156, v156\n\t" \
  "v_exp_f32 v157, v157\n\t" \
  "v_smfmac_f32_32x32x32_bf16 v[80:95], v[228:231], v[144:151], v236\n\t" \
  "v_exp_f32 v158, v158\n\t" \
  "v_exp_f32 v159, v159\n\t" \
  "v_exp_f32 v128, v128\n\t" \
  "v_exp_f32 v129, v129\n\t" \
  "v_smfmac_f32_32x32x32_bf16 v[80:95], v[232:235], v[152:159], v236\n\t" \
  "s_sub_u32 s63, s63, 1\n\t" \
  "ds_read_b32 v247, v245 offset:8\n\t" \
  "v_exp_f32 v130, v130\n\t" \
  "v_exp_f32 v131, v131\n\t" \
  "v_exp_f32 v132, v132\n\t" \
  "v_exp_f32 v133, v133\n\t" \
  "v_mfma_f32_32x32x16_bf16 v[144:159], v[212:215], v[188:191], 0\n\t" \
  "v_exp_f32 v134, v134\n\t" \
  "v_exp_f32 v135, v135\n\t" \
  "v_exp_f32 v136, v136\n\t" \
  "v_exp_f32 v137, v137\n\t" \
  "v_mfma_f32_32x32x8_bf16 v[144:159], v[218:219], v[206:207], v[144:159]\n\t" \
  "v_exp_f32 v138, v138\n\t" \
  "v_exp_f32 v139, v139\n\t" \
  "v_exp_f32 v140, v140\n\t" \
  "v_exp_f32 v141, v141\n\t" \
  "v_smfmac_f32_32x32x32_bf16 v[96:111], v[228:231], v[128:135], v236\n\t" \
  "global_load_dwordx4 v[212:215], v237, s[40:41] offset:-3584 sc1\n\t" \
  "global_load_dwordx2 v[218:219], v238, s[40:41] offset:-512 sc1\n\t" \
  "v_exp_f32 v142, v142\n\t" \
  "v_exp_f32 v143, v143\n\t" \
  "v_exp_f32 v144, v144\n\t" \
  "v_exp_f32 v145, v145\n\t" \
  "v_smfmac_f32_32x32x32_bf16 v[96:111], v[232:235], v[136:143], v236\n\t" \
  "s_waitcnt vmcnt(4) lgkmcnt(0)\n\t" \
  "v_exp_f32 v146, v146\n\t" \
  "v_exp_f32 v147, v147\n\t" \
  "v_exp_f32 v148, v148\n\t" \
  "v_exp_f32 v149, v149\n\t" \
  "v_mfma_f32_32x32x16_bf16 v[128:143], v[208:211], v[160:163], 0\n\t" \
  "v_exp_f32 v150, v150\n\t" \
  "v_exp_f32 v151, v151\n\t" \
  "v_exp_f32 v152, v152\n\t" \
  "v_exp_f32 v153, v153\n\t" \
  "v_mfma_f32_32x32x8_bf16 v[128:143], v[216:217], v[192:193], v[128:143]\n\t" \
  "v_exp_f32 v154, v154\n\t" \
  "v_exp_f32 v155, v155\n\t" \
  "v_exp_f32 v156, v156\n\t" \
  "v_exp_f32 v157, v157\n\t" \
  "v_smfmac_f32_32x32x32_bf16 v[112:127], v[228:231], v[144:151], v236\n\t" \
  "global_load_dwordx4 v[228:231], v237, s[40:41] offset:-2560 sc1\n\t" \
  "s_cbranch_scc0 .Lattn%=\n\t" \
  ".Lalast%=:\n\t" \
  "v_cmp_gt_u32 vcc, 8, v247\n\t" \
  "s_nop 1\n\t" \
  "s_cbranch_vccnz .Laqw%=\n\t" \
  "v_exp_f32 v158, v158\n\t" \
  "v_exp_f32 v159, v159\n\t" \
  "v_exp_f32 v128, v128\n\t" \
  "v_exp_f32 v129, v129\n\t" \
  "v_smfmac_f32_32x32x32_bf16 v[112:127], v[232:235], v[152:159], v236\n\t" \
  "global_load_dwordx4 v[232:235], v237, s[40:41] offset:-1536 sc1\n\t" \
  "s_waitcnt vmcnt(4)\n\t" \
  "v_exp_f32 v130, v130\n\t" \
  "v_exp_f32 v131, v131\n\t" \
  "v_exp_f32 v132, v132\n\t" \
  "v_exp_f32 v133, v133\n\t" \
  "v_mfma_f32_32x32x16_bf16 v[144:159], v[208:211], v[164:167], 0\n\t" \
  "v_exp_f32 v134, v134\n\t" \
  "v_exp_f32 v135, v135\n\t" \
  "v_exp_f32 v136, v136\n\t" \
  "v_exp_f32 v137, v137\n\t" \
  "v_mfma_f32_32x32x8_bf16 v[144:159], v[216:217], v[194:195], v[144:159]\n\t" \
  "v_exp_f32 v138, v138\n\t" \
  "v_exp_f32 v139, v139\n\t" \
  "v_exp_f32 v140, v140\n\t" \
  "v_exp_f32 v141, v141\n\t" \
  "v_smfmac_f32_32x32x32_bf16 v[0:15], v[220:223], v[128:135], v236\n\t" \
  "v_exp_f32 v142, v142\n\t" \
  "v_exp_f32 v143, v143\n\t" \
  "v_exp_f32 v144, v144\n\t" \
  "v_exp_f32 v145, v145\n\t" \
  "v_smfmac_f32_32x32x32_bf16 v[0:15], v[224:227], v[136:143], v236\n\t" \
  "v_exp_f32 v146, v146\n\t" \
  "v_exp_f32 v147, v147\n\t" \
  "v_exp_f32 v148, v148\n\t" \
  "v_exp_f32 v149, v149\n\t" \
  "v_mfma_f32_32x32x16_bf16 v[128:143], v[208:211], v[168:171], 0\n\t" \
  "v_exp_f32 v150, v150\n\t" \
  "v_exp_f32 v151, v151\n\t" \
  "v_exp_f32 v152, v152\n\t" \
  "v_exp_f32 v153, v153\n\t" \
  "v_mfma_f32_32x32x8_bf16 v[128:143], v[216:217], v[196:197], v[128:143]\n\t" \
  "v_exp_f32 v154, v154\n\t" \
  "v_exp_f32 v155, v155\n\t" \
  "v_exp_f32 v156, v156\n\t" \
  "v_exp_f32 v157, v157\n\t" \
  "v_smfmac_f32_32x32x32_bf16 v[16:31], v[220:223], v[144:151], v236\n\t" \
  "v_exp_f32 v158, v158\n\t" \
  "v_exp_f32 v159, v159\n\t" \
  "v_exp_f32 v128, v128\n\t" \
  "v_exp_f32 v129, v129\n\t" \
  "v_smfmac_f32_32x32x32_bf16 v[16:31], v[224:227], v[152:159], v236\n\t" \
  "v_exp_f32 v130, v130\n\t" \
  "v_exp_f32 v131, v131\n\t" \
  "v_exp_f32 v132, v132\n\t" \
  "v_exp_f32 v133, v133\n\t" \
  "v_mfma_f32_32x32x16_bf16 v[144:159], v[208:211], v[172:175], 0\n\t" \
  "v_exp_f32 v134, v134\n\t" \
  "v_exp_f32 v135, v135\n\t" \
  "v_exp_f32 v136, v136\n\t" \
  "v_exp_f32 v137, v137\n\t" \
  "v_mfma_f32_32x32x8_bf16 v[144:159], v[216:217], v[198:199], v[144:159]\n\t" \
  "v_exp_f32 v138, v138\n\t" \
  "v_exp_f32 v139, v139\n\t" \
  "v_exp_f32 v140, v140\n\t" \
  "v_exp_f32 v141, v141\n\t" \
  "v_smfmac_f32_32x32x32_bf16 v[32:47], v[220:223], v[128:135], v236\n\t" \
  "v_exp_f32 v142, v142\n\t" \
  "v_exp_f32 v143, v143\n\t" \
  "v_exp_f32 v144, v144\n\t" \
  "v_exp_f32 v145, v145\n\t" \
  "v_smfmac_f32_32x32x32_bf16 v[32:47], v[224:227], v[136:143], v236\n\t" \
  "s_waitcnt vmcnt(2)\n\t" \
  "v_exp_f32 v146, v146\n\t" \
  "v_exp_f32 v147, v147\n\t" \
  "v_exp_f32 v148, v148\n\t" \
  "v_exp_f32 v149, v149\n\t" \
  "v_mfma_f32_32x32x16_bf16 v[128:143], v[212:215], v[160:163], 0\n\t" \
  "v_exp_f32 v150, v150\n\t" \
  "v_exp_f32 v151, v151\n\t" \
  "v_exp_f32 v152, v152\n\t" \
  "v_exp_f32 v153, v153\n\t" \
  "v_mfma_f32_32x32x8_bf16 v[128:143], v[218:219], v[192:193], v[128:143]\n\t" \
  "v_exp_f32 v154, v154\n\t" \
  "v_exp_f32 v155, v155\n\t" \
  "v_exp_f32 v156, v156\n\t" \
  "v_exp_f32 v157, v157\n\t" \
  "v_smfmac_f32_32x32x32_bf16 v[48:63], v[220:223], v[144:151], v236\n\t" \
  "v_exp_f32 v158, v158\n\t" \
  "v_exp_f32 v159, v159\n\t" \
  "v_exp_f32 v128, v128\n\t" \
  "v_exp_f32 v129, v129\n\t" \
  "v_smfmac_f32_32x32x32_bf16 v[48:63], v[224:227], v[152:159], v236\n\t" \
  "s_waitcnt vmcnt(0)\n\t" \
  "v_exp_f32 v130, v130\n\t" \
  "v_exp_f32 v131, v131\n\t" \
  "v_exp_f32 v132, v132\n\t" \
  "v_exp_f32 v133, v133\n\t" \
  "v_mfma_f32_32x32x16_bf16 v[144:159], v[212:215], v[164:167], 0\n\t" \
  "v_exp_f32 v134, v134\n\t" \
  "v_exp_f32 v135, v135\n\t" \
  "v_exp_f32 v136, v136\n\t" \
  "v_exp_f32 v137, v137\n\t" \
  "v_mfma_f32_32x32x8_bf16 v[144:159], v[218:219], v[194:195], v[144:159]\n\t" \
  "v_exp_f32 v138, v138\n\t" \
  "v_exp_f32 v139, v139\n\t" \
  "v_exp_f32 v140, v140\n\t" \
  "v_exp_f32 v141, v141\n\t" \
  "v_smfmac_f32_32x32x32_bf16 v[0:15], v[228:231], v[128:135], v236\n\t" \
  "v_exp_f32 v142, v142\n\t" \
  "v_exp_f32 v143, v143\n\t" \
  "v_exp_f32 v144, v144\n\t" \
  "v_exp_f32 v145, v145\n\t" \
  "v_smfmac_f32_32x32x32_bf16 v[0:15], v[232:235], v[136:143], v236\n\t" \
  "v_exp_f32 v146, v146\n\t" \
  "v_exp_f32 v147, v147\n\t" \
  "v_exp_f32 v148, v148\n\t" \
  "v_exp_f32 v149, v149\n\t" \
  "v_mfma_f32_32x32x16_bf16 v[128:143], v[212:215], v[168:171], 0\n\t" \
  "v_exp_f32 v150, v150\n\t" \
  "v_exp_f32 v151, v151\n\t" \
  "v_exp_f32 v152, v152\n\t" \
  "v_exp_f32 v153, v153\n\t" \
  "v_mfma_f32_32x32x8_bf16 v[128:143], v[218:219], v[196:197], v[128:143]\n\t" \
  "v_exp_f32 v154, v154\n\t" \
  "v_exp_f32 v155, v155\n\t" \
  "v_exp_f32 v156, v156\n\t" \
  "v_exp_f32 v157, v157\n\t" \
  "v_smfmac_f32_32x32x32_bf16 v[16:31], v[228:231], v[144:151], v236\n\t" \
  "ds_write_b128 v241, v[0:3] offset:0\n\t" \
  "v_exp_f32 v158, v158\n\t" \
  "v_exp_f32 v159, v159\n\t" \
  "v_exp_f32 v128, v128\n\t" \
  "v_exp_f32 v129, v129\n\t" \
  "v_smfmac_f32_32x32x32_bf16 v[16:31], v[232:235], v[152:159], v236\n\t" \
  "ds_write_b128 v241, v[4:7] offset:32\n\t" \
  "v_exp_f32 v130, v130\n\t" \
  "v_exp_f32 v131, v131\n\t" \
  "v_exp_f32 v132, v132\n\t" \
  "v_exp_f32 v133, v133\n\t" \
  "v_mfma_f32_32x32x16_bf16 v[144:159], v[212:215], v[172:175], 0\n\t" \
  "ds_write_b128 v240, v[8:11]\n\t" \
  "v_add_u32 v240, v242, v240\n\t" \
  "v_exp_f32 v134, v134\n\t" \
  "v_exp_f32 v135, v135\n\t" \
  "v_exp_f32 v136, v136\n\t" \
  "v_exp_f32 v137, v137\n\t" \
  "v_mfma_f32_32x32x8_bf16 v[144:159], v[218:219], v[198:199], v[144:159]\n\t" \
  "v_exp_f32 v138, v138\n\t" \
  "v_exp_f32 v139, v139\n\t" \
  "v_exp_f32 v140, v140\n\t" \
  "v_exp_f32 v141, v141\n\t" \
  "v_smfmac_f32_32x32x32_bf16 v[32:47], v[228:231], v[128:135], v236\n\t" \
  "ds_write_b128 v241, v[16:19] offset:10304\n\t" \
  "v_exp_f32 v142, v142\n\t" \
  "v_exp_f32 v143, v143\n\t" \
  "v_exp_f32 v144, v144\n\t" \
  "v_exp_f32 v145, v145\n\t" \
  "v_smfmac_f32_32x32x32_bf16 v[32:47], v[232:235], v[136:143], v236\n\t" \
  "ds_write_b128 v241, v[20:23] offset:10336\n\t" \
  "v_exp_f32 v146, v146\n\t" \
  "v_exp_f32 v147, v147\n\t" \
  "v_exp_f32 v148, v148\n\t" \
  "v_exp_f32 v149, v149\n\t" \
  "v_mfma_f32_32x32x16_bf16 v[128:143], v[208:211], v[176:179], 0\n\t" \
  "ds_write_b128 v240, v[24:27]\n\t" \
  "v_add_u32 v240, v242, v240\n\t" \
  "v_exp_f32 v150, v150\n\t" \
  "v_exp_f32 v151, v151\n\t" \
  "v_exp_f32 v152, v152\n\t" \
  "v_exp_f32 v153, v153\n\t" \
  "v_mfma_f32_32x32x8_bf16 v[128:143], v[216:217], v[200:201], v[128:143]\n\t" \
  "v_exp_f32 v154, v154\n\t" \
  "v_exp_f32 v155, v155\n\t" \
  "v_exp_f32 v156, v156\n\t" \
  "v_exp_f32 v157, v157\n\t" \
  "v_smfmac_f32_32x32x32_bf16 v[48:63], v[228:231], v[144:151], v236\n\t" \
  "ds_write_b128 v241, v[32:35] offset:20608\n\t" \
  "v_exp_f32 v158, v158\n\t" \
  "v_exp_f32 v159, v159\n\t" \
  "v_exp_f32 v128, v128\n\t" \
  "v_exp_f32 v129, v129\n\t" \
  "v_smfmac_f32_32x32x32_bf16 v[48:63], v[232:235], v[152:159], v236\n\t" \
  "ds_write_b128 v241, v[36:39] offset:20640\n\t" \
  "v_exp_f32 v130, v130\n\t" \
  "v_exp_f32 v131, v131\n\t" \
  "v_exp_f32 v132, v132\n\t" \
  "v_exp_f32 v133, v133\n\t" \
  "v_mfma_f32_32x32x16_bf16 v[144:159], v[208:211], v[180:183], 0\n\t" \
  "ds_write_b128 v240, v[40:43]\n\t" \
  "v_add_u32 v240, v242, v240\n\t" \
  "v_exp_f32 v134, v134\n\t" \
  "v_exp_f32 v135, v135\n\t" \
  "v_exp_f32 v136, v136\n\t" \
  "v_exp_f32 v137, v137\n\t" \
  "v_mfma_f32_32x32x8_bf16 v[144:159], v[216:217], v[202:203], v[144:159]\n\t" \
  "v_exp_f32 v138, v138\n\t" \
  "v_exp_f32 v139, v139\n\t" \
  "v_exp_f32 v140, v140\n\t" \
  "v_exp_f32 v141, v141\n\t" \
  "v_smfmac_f32_32x32x32_bf16 v[64:79], v[220:223], v[128:135], v236\n\t" \
  "ds_write_b128 v241, v[48:51] offset:30912\n\t" \
  "v_exp_f32 v142, v142\n\t" \
  "v_exp_f32 v143, v143\n\t" \
  "v_exp_f32 v144, v144\n\t" \
  "v_exp_f32 v145, v145\n\t" \
  "v_smfmac_f32_32x32x32_bf16 v[64:79], v[224:227], v[136:143], v236\n\t" \
  "ds_write_b128 v241, v[52:55] offset:30944\n\t" \
  "v_exp_f32 v146, v146\n\t" \
  "v_exp_f32 v147, v147\n\t" \
  "v_exp_f32 v148, v148\n\t" \
  "v_exp_f32 v149, v149\n\t" \
  "v_mfma_f32_32x32x16_bf16 v[128:143], v[208:211], v[184:187], 0\n\t" \
  "ds_write_b128 v240, v[56:59]\n\t" \
  "v_add_u32 v240, v242, v240\n\t" \
  "v_exp_f32 v150, v150\n\t" \
  "v_exp_f32 v151, v151\n\t" \
  "v_exp_f32 v152, v152\n\t" \
  "v_exp_f32 v153, v153\n\t" \
  "v_mfma_f32_32x32x8_bf16 v[128:143], v[216:217], v[204:205], v[128:143]\n\t" \
  "v_exp_f32 v154, v154\n\t" \
  "v_exp_f32 v155, v155\n\t" \
  "v_exp_f32 v156, v156\n\t" \
  "v_exp_f32 v157, v157\n\t" \
  "v_smfmac_f32_32x32x32_bf16 v[80:95], v[220:223], v[144:151], v236\n\t" \
  "v_exp_f32 v158, v158\n\t" \
  "v_exp_f32 v159, v159\n\t" \
  "v_exp_f32 v128, v128\n\t" \
  "v_exp_f32 v129, v129\n\t" \
  "v_smfmac_f32_32x32x32_bf16 v[80:95], v[224:227], v[152:159], v236\n\t" \
  "s_waitcnt lgkmcnt(0)\n\t" \
  "s_barrier\n\t" \
  "v_exp_f32 v130, v130\n\t" \
  "v_exp_f32 v131, v131\n\t" \
  "v_exp_f32 v132, v132\n\t" \
  "v_exp_f32 v133, v133\n\t" \
  "v_mfma_f32_32x32x16_bf16 v[144:159], v[208:211], v[188:191], 0\n\t" \
  "v_exp_f32 v134, v134\n\t" \
  "v_exp_f32 v135, v135\n\t" \
  "v_exp_f32 v136, v136\n\t" \
  "v_exp_f32 v137, v137\n\t" \
  "v_mfma_f32_32x32x8_bf16 v[144:159], v[216:217], v[206:207], v[144:159]\n\t" \
  "v_exp_f32 v138, v138\n\t" \
  "v_exp_f32 v139, v139\n\t" \
  "v_exp_f32 v140, v140\n\t" \
  "v_exp_f32 v141, v141\n\t" \
  "v_smfmac_f32_32x32x32_bf16 v[96:111], v[220:223], v[128:135], v236\n\t" \
  "v_exp_f32 v142, v142\n\t" \
  "v_exp_f32 v143, v143\n\t" \
  "v_exp_f32 v144, v144\n\t" \
  "v_exp_f32 v145, v145\n\t" \
  "v_smfmac_f32_32x32x32_bf16 v[96:111], v[224:227], v[136:143], v236\n\t" \
  "v_exp_f32 v146, v146\n\t" \
  "v_exp_f32 v147, v147\n\t" \
  "v_exp_f32 v148, v148\n\t" \
  "v_exp_f32 v149, v149\n\t" \
  "v_mfma_f32_32x32x16_bf16 v[128:143], v[212:215], v[176:179], 0\n\t" \
  "v_exp_f32 v150, v150\n\t" \
  "v_exp_f32 v151, v151\n\t" \
  "v_exp_f32 v152, v152\n\t" \
  "v_exp_f32 v153, v153\n\t" \
  "v_mfma_f32_32x32x8_bf16 v[128:143], v[218:219], v[200:201], v[128:143]\n\t" \
  "v_exp_f32 v154, v154\n\t" \
  "v_exp_f32 v155, v155\n\t" \
  "v_exp_f32 v156, v156\n\t" \
  "v_exp_f32 v157, v157\n\t" \
  "v_smfmac_f32_32x32x32_bf16 v[112:127], v[220:223], v[144:151], v236\n\t" \
  "v_exp_f32 v158, v158\n\t" \
  "v_exp_f32 v159, v159\n\t" \
  "v_exp_f32 v128, v128\n\t" \
  "v_exp_f32 v129, v129\n\t" \
  "v_smfmac_f32_32x32x32_bf16 v[112:127], v[224:227], v[152:159], v236\n\t" \
  "v_exp_f32 v130, v130\n\t" \
  "v_exp_f32 v131, v131\n\t" \
  "v_exp_f32 v132, v132\n\t" \
  "v_exp_f32 v133, v133\n\t" \
  "v_mfma_f32_32x32x16_bf16 v[144:159], v[212:215], v[180:183], 0\n\t" \
  "v_exp_f32 v134, v134\n\t" \
  "v_exp_f32 v135, v135\n\t" \
  "v_exp_f32 v136, v136\n\t" \
  "v_exp_f32 v137, v137\n\t" \
  "v_mfma_f32_32x32x8_bf16 v[144:159], v[218:219], v[202:203], v[144:159]\n\t" \
  "v_exp_f32 v138, v138\n\t" \
  "v_exp_f32 v139, v139\n\t" \
  "v_exp_f32 v140, v140\n\t" \
  "v_exp_f32 v141, v141\n\t" \
  "v_smfmac_f32_32x32x32_bf16 v[64:79], v[228:231], v[128:135], v236\n\t" \
  "v_exp_f32 v142, v142\n\t" \
  "v_exp_f32 v143, v143\n\t" \
  "v_exp_f32 v144, v144\n\t" \
  "v_exp_f32 v145, v145\n\t" \
  "v_smfmac_f32_32x32x32_bf16 v[64:79], v[232:235], v[136:143], v236\n\t" \
  "v_exp_f32 v146, v146\n\t" \
  "v_exp_f32 v147, v147\n\t" \
  "v_exp_f32 v148, v148\n\t" \
  "v_exp_f32 v149, v149\n\t" \
  "v_mfma_f32_32x32x16_bf16 v[128:143], v[212:215], v[184:187], 0\n\t" \
  "v_exp_f32 v150, v150\n\t" \
  "v_exp_f32 v151, v151\n\t" \
  "v_exp_f32 v152, v152\n\t" \
  "v_exp_f32 v153, v153\n\t" \
  "v_mfma_f32_32x32x8_bf16 v[128:143], v[218:219], v[204:205], v[128:143]\n\t" \
  "v_exp_f32 v154, v154\n\t" \
  "v_exp_f32 v155, v155\n\t" \
  "v_exp_f32 v156, v156\n\t" \
  "v_exp_f32 v157, v157\n\t" \
  "v_smfmac_f32_32x32x32_bf16 v[80:95], v[228:231], v[144:151], v236\n\t" \
  "ds_write_b128 v243, v[64:67] offset:0\n\t" \
  "v_exp_f32 v158, v158\n\t" \
  "v_exp_f32 v159, v159\n\t" \
  "v_exp_f32 v128, v128\n\t" \
  "v_exp_f32 v129, v129\n\t" \
  "v_smfmac_f32_32x32x32_bf16 v[80:95], v[232:235], v[152:159], v236\n\t" \
  "ds_write_b128 v243, v[68:71] offset:32\n\t" \
  "v_exp_f32 v130, v130\n\t" \
  "v_exp_f32 v131, v131\n\t" \
  "v_exp_f32 v132, v132\n\t" \
  "v_exp_f32 v133, v133\n\t" \
  "v_mfma_f32_32x32x16_bf16 v[144:159], v[212:215], v[188:191], 0\n\t" \
  "ds_write_b128 v240, v[72:75]\n\t" \
  "v_add_u32 v240, v242, v240\n\t" \
  "v_exp_f32 v134, v134\n\t" \
  "v_exp_f32 v135, v135\n\t" \
  "v_exp_f32 v136, v136\n\t" \
  "v_exp_f32 v137, v137\n\t" \
  "v_mfma_f32_32x32x8_bf16 v[144:159], v[218:219], v[206:207], v[144:159]\n\t" \
  "v_exp_f32 v138, v138\n\t" \
  "v_exp_f32 v139, v139\n\t" \
  "v_exp_f32 v140, v140\n\t" \
  "v_exp_f32 v141, v141\n\t" \
  "v_smfmac_f32_32x32x32_bf16 v[96:111], v[228:231], v[128:135], v236\n\t" \
  "ds_write_b128 v243, v[80:83] offset:10304\n\t" \
  "v_exp_f32 v142, v142\n\t" \
  "v_exp_f32 v143, v143\n\t" \
  "v_exp_f32 v144, v144\n\t" \
  "v_exp_f32 v145, v145\n\t" \
  "v_smfmac_f32_32x32x32_bf16 v[96:111], v[232:235], v[136:143], v236\n\t" \
  "ds_write_b128 v243, v[84:87] offset:10336\n\t" \
  "v_exp_f32 v146, v146\n\t" \
  "v_exp_f32 v147, v147\n\t" \
  "v_exp_f32 v148, v148\n\t" \
  "v_exp_f32 v149, v149\n\t" \
  "ds_write_b128 v240, v[88:91]\n\t" \
  "v_add_u32 v240, v242, v240\n\t" \
  "v_exp_f32 v150, v150\n\t" \
  "v_exp_f32 v151, v151\n\t" \
  "v_exp_f32 v152, v152\n\t" \
  "v_exp_f32 v153, v153\n\t" \
  "v_exp_f32 v154, v154\n\t" \
  "v_exp_f32 v155, v155\n\t" \
  "v_exp_f32 v156, v156\n\t" \
  "v_exp_f32 v157, v157\n\t" \
  "v_smfmac_f32_32x32x32_bf16 v[112:127], v[228:231], v[144:151], v236\n\t" \
  "ds_write_b128 v243, v[96:99] offset:20608\n\t" \
  "v_exp_f32 v158, v158\n\t" \
  "v_exp_f32 v159, v159\n\t" \
  "s_nop 1\n\t" \
  "v_smfmac_f32_32x32x32_bf16 v[112:127], v[232:235], v[152:159], v236\n\t" \
  "ds_write_b128 v243, v[100:103] offset:20640\n\t" \
  "ds_write_b128 v240, v[104:107]\n\t" \
  "v_add_u32 v240, v242, v240\n\t" \
  "s_mul_i32 s63, s62, 10240\n\t" \
  "s_lshl_b32 s64, s32, 10\n\t" \
  "s_add_u32 s63, s63, s64\n\t" \
  "s_add_u32 s63, s63, 10529792\n\t" \
  "s_add_u32 s44, s24, s63\n\t" \
  "s_addc_u32 s45, s25, 0\n\t" \
  "v_mov_b32 v80, 808\n\t" \
  "s_cmp_eq_u32 s32, 0\n\t" \
  "s_nop 0\n\t" \
  "ds_write_b128 v243, v[112:115] offset:30912\n\t" \
  "ds_write_b128 v243, v[116:119] offset:30944\n\t" \
  "ds_write_b128 v240, v[120:123]\n\t" \
  "s_setprio 0\n\t" \
  "s_waitcnt lgkmcnt(0)\n\t" \
  "s_barrier\n\t" \
  "ds_read2st64_b32 v[32:33], v251 offset0:0 offset1:2\n\t" \
  "ds_read2st64_b32 v[34:35], v251 offset0:4 offset1:6\n\t" \
  "ds_read2st64_b32 v[36:37], v253 offset0:0 offset1:2\n\t" \
  "ds_read2st64_b32 v[38:39], v253 offset0:4 offset1:6\n\t" \
  "ds_read_b128 v[0:3], v249 offset:0\n\t" \
  "ds_read_b128 v[4:7], v249 offset:2560\n\t" \
  "ds_read_b128 v[8:11], v249 offset:5120\n\t" \
  "ds_read_b128 v[12:15], v249 offset:7680\n\t" \
  "ds_read_b128 v[16:19], v249 offset:64\n\t" \
  "ds_read_b128 v[20:23], v249 offset:2624\n\t" \
  "ds_read_b128 v[24:27], v249 offset:5184\n\t" \
  "ds_read_b128 v[28:31], v249 offset:7744\n\t" \
  "s_cbranch_scc0 .Lac2%=\n\t" \
  "ds_read2st64_b32 v[72:73], v246 offset0:0 offset1:2\n\t" \
  "ds_read2st64_b32 v[74:75], v246 offset0:4 offset1:6\n\t" \
  "ds_read2st64_b32 v[76:77], v248 offset0:0 offset1:2\n\t" \
  "s_waitcnt lgkmcnt(11)\n\t" \
  "v_pk_add_f32 v[32:33], v[32:33], v[34:35]\n\t" \
  "v_pk_add_f32 v[36:37], v[36:37], v[38:39]\n\t" \
  "v_add_f32 v32, v32, v33\n\t" \
  "v_add_f32 v36, v36, v37\n\t" \
  "v_rcp_f32 v34, v32\n\t" \
  "v_rcp_f32 v38, v36\n\t" \
  "v_subrev_u32 v32, 0x21800000, v32\n\t" \
  "v_subrev_u32 v36, 0x21800000, v36\n\t" \
  "v_max_u32 v32, v32, v36\n\t" \
  "v_cmp_ge_u32 vcc, s43, v32\n\t" \
  "ds_read2st64_b32 v[78:79], v248 offset0:4 offset1:6\n\t" \
  "ds_read_b128 v[40:43], v244 offset:0\n\t" \
  "ds_read_b128 v[44:47], v244 offset:2560\n\t" \
  "ds_read_b128 v[48:51], v244 offset:5120\n\t" \
  "ds_read_b128 v[52:55], v244 offset:7680\n\t" \
  "ds_read_b128 v[56:59], v244 offset:64\n\t" \
  "ds_read_b128 v[60:63], v244 offset:2624\n\t" \
  "ds_read_b128 v[64:67], v244 offset:5184\n\t" \
  "ds_read_b128 v[68:71], v244 offset:7744\n\t" \
  "s_waitcnt lgkmcnt(15)\n\t" \
  "v_pk_add_f32 v[0:1], v[0:1], v[4:5]\n\t" \
  "v_pk_add_f32 v[2:3], v[2:3], v[6:7]\n\t" \
  "v_pk_add_f32 v[8:9], v[8:9], v[12:13]\n\t" \
  "v_pk_add_f32 v[10:11], v[10:11], v[14:15]\n\t" \
  "v_pk_add_f32 v[0:1], v[0:1], v[8:9]\n\t" \
  "v_pk_add_f32 v[2:3], v[2:3], v[10:11]\n\t" \
  "v_pk_mul_f32 v[0:1], v[34:35], v[0:1] op_sel_hi:[0,1]\n\t" \
  "v_pk_mul_f32 v[2:3], v[34:35], v[2:3] op_sel_hi:[0,1]\n\t" \
  "v_cvt_pk_bf16_f32 v4, v0, v1\n\t" \
  "v_cvt_pk_bf16_f32 v5, v2, v3\n\t" \
  "s_waitcnt lgkmcnt(12)\n\t" \
  "v_pk_add_f32 v[16:17], v[16:17], v[20:21]\n\t" \
  "v_pk_add_f32 v[18:19], v[18:19], v[22:23]\n\t" \
  "v_pk_add_f32 v[24:25], v[24:25], v[28:29]\n\t" \
  "v_pk_add_f32 v[26:27], v[26:27], v[30:31]\n\t" \
  "v_pk_add_f32 v[16:17], v[16:17], v[24:25]\n\t" \
  "v_pk_add_f32 v[18:19], v[18:19], v[26:27]\n\t" \
  "v_pk_mul_f32 v[16:17], v[38:39], v[16:17] op_sel_hi:[0,1]\n\t" \
  "v_pk_mul_f32 v[18:19], v[38:39], v[18:19] op_sel_hi:[0,1]\n\t" \
  "v_cvt_pk_bf16_f32 v6, v16, v17\n\t" \
  "v_cvt_pk_bf16_f32 v7, v18, v19\n\t" \
  "s_and_b64 exec, vcc, s[50:51]\n\t" \
  "global_store_dwordx4 v237, v[4:7], s[44:45] offset:-2048 sc1\n\t" \
  "s_mov_b64 exec, -1\n\t" \
  "s_andn2_b64 s[50:51], s[50:51], vcc\n\t" \
  "s_waitcnt lgkmcnt(8)\n\t" \
  "v_pk_add_f32 v[72:73], v[72:73], v[74:75]\n\t" \
  "v_pk_add_f32 v[76:77], v[76:77], v[78:79]\n\t" \
  "v_add_f32 v72, v72, v73\n\t" \
  "v_add_f32 v76, v76, v77\n\t" \
  "v_rcp_f32 v74, v72\n\t" \
  "v_rcp_f32 v78, v76\n\t" \
  "v_subrev_u32 v72, 0x21800000, v72\n\t" \
  "v_subrev_u32 v76, 0x21800000, v76\n\t" \
  "v_max_u32 v72, v72, v76\n\t" \
  "v_cmp_ge_u32 vcc, s43, v72\n\t" \
  "s_waitcnt lgkmcnt(4)\n\t" \
  "v_pk_add_f32 v[40:41], v[40:41], v[44:45]\n\t" \
  "v_pk_add_f32 v[42:43], v[42:43], v[46:47]\n\t" \
  "v_pk_add_f32 v[48:49], v[48:49], v[52:53]\n\t" \
  "v_pk_add_f32 v[50:51], v[50:51], v[54:55]\n\t" \
  "v_pk_add_f32 v[40:41], v[40:41], v[48:49]\n\t" \
  "v_pk_add_f32 v[42:43], v[42:43], v[50:51]\n\t" \
  "v_pk_mul_f32 v[40:41], v[74:75], v[40:41] op_sel_hi:[0,1]\n\t" \
  "v_pk_mul_f32 v[42:43], v[74:75], v[42:43] op_sel_hi:[0,1]\n\t" \
  "v_cvt_pk_bf16_f32 v44, v40, v41\n\t" \
  "v_cvt_pk_bf16_f32 v45, v42, v43\n\t" \
  "s_waitcnt lgkmcnt(0)\n\t" \
  "v_pk_add_f32 v[56:57], v[56:57], v[60:61]\n\t" \
  "v_pk_add_f32 v[58:59], v[58:59], v[62:63]\n\t" \
  "v_pk_add_f32 v[64:65], v[64:65], v[68:69]\n\t" \
  "v_pk_add_f32 v[66:67], v[66:67], v[70:71]\n\t" \
  "v_pk_add_f32 v[56:57], v[56:57], v[64:65]\n\t" \
  "v_pk_add_f32 v[58:59], v[58:59], v[66:67]\n\t" \
  "v_pk_mul_f32 v[56:57], v[78:79], v[56:57] op_sel_hi:[0,1]\n\t" \
  "v_pk_mul_f32 v[58:59], v[78:79], v[58:59] op_sel_hi:[0,1]\n\t" \
  "v_cvt_pk_bf16_f32 v46, v56, v57\n\t" \
  "v_cvt_pk_bf16_f32 v47, v58, v59\n\t" \
  "s_and_b64 exec, vcc, s[52:53]\n\t" \
  "global_store_dwordx4 v237, v[44:47], s[44:45] offset:2048 sc1\n\t" \
  "s_mov_b64 exec, -1\n\t" \
  "s_andn2_b64 s[52:53], s[52:53], vcc\n\t" \
  "s_or_b64 vcc, s[50:51], s[52:53]\n\t" \
  "s_branch .Lac3%=\n\t" \
  ".Lac2%=:\n\t" \
  "s_waitcnt lgkmcnt(8)\n\t" \
  "v_pk_add_f32 v[32:33], v[32:33], v[34:35]\n\t" \
  "v_pk_add_f32 v[36:37], v[36:37], v[38:39]\n\t" \
  "v_add_f32 v32, v32, v33\n\t" \
  "v_add_f32 v36, v36, v37\n\t" \
  "v_rcp_f32 v34, v32\n\t" \
  "v_rcp_f32 v38, v36\n\t" \
  "v_subrev_u32 v32, 0x21800000, v32\n\t" \
  "v_subrev_u32 v36, 0x21800000, v36\n\t" \
  "v_max_u32 v32, v32, v36\n\t" \
  "v_cmp_ge_u32 vcc, s43, v32\n\t" \
  "s_waitcnt lgkmcnt(4)\n\t" \
  "v_pk_add_f32 v[0:1], v[0:1], v[4:5]\n\t" \
  "v_pk_add_f32 v[2:3], v[2:3], v[6:7]\n\t" \
  "v_pk_add_f32 v[8:9], v[8:9], v[12:13]\n\t" \
  "v_pk_add_f32 v[10:11], v[10:11], v[14:15]\n\t" \
  "v_pk_add_f32 v[0:1], v[0:1], v[8:9]\n\t" \
  "v_pk_add_f32 v[2:3], v[2:3], v[10:11]\n\t" \
  "v_pk_mul_f32 v[0:1], v[34:35], v[0:1] op_sel_hi:[0,1]\n\t" \
  "v_pk_mul_f32 v[2:3], v[34:35], v[2:3] op_sel_hi:[0,1]\n\t" \
  "v_cvt_pk_bf16_f32 v4, v0, v1\n\t" \
  "v_cvt_pk_bf16_f32 v5, v2, v3\n\t" \
  "s_waitcnt lgkmcnt(0)\n\t" \
  "v_pk_add_f32 v[16:17], v[16:17], v[20:21]\n\t" \
  "v_pk_add_f32 v[18:19], v[18:19], v[22:23]\n\t" \
  "v_pk_add_f32 v[24:25], v[24:25], v[28:29]\n\t" \
  "v_pk_add_f32 v[26:27], v[26:27], v[30:31]\n\t" \
  "v_pk_add_f32 v[16:17], v[16:17], v[24:25]\n\t" \
  "v_pk_add_f32 v[18:19], v[18:19], v[26:27]\n\t" \
  "v_pk_mul_f32 v[16:17], v[38:39], v[16:17] op_sel_hi:[0,1]\n\t" \
  "v_pk_mul_f32 v[18:19], v[38:39], v[18:19] op_sel_hi:[0,1]\n\t" \
  "v_cvt_pk_bf16_f32 v6, v16, v17\n\t" \
  "v_cvt_pk_bf16_f32 v7, v18, v19\n\t" \
  "s_and_b64 exec, vcc, s[50:51]\n\t" \
  "global_store_dwordx4 v237, v[4:7], s[44:45] offset:-2048 sc1\n\t" \
  "s_mov_b64 exec, -1\n\t" \
  "s_andn2_b64 s[50:51], s[50:51], vcc\n\t" \
  ".Lac3%=:\n\t" \
  "s_cbranch_scc0 .Laok%=\n\t" \
  "v_mov_b32 v81, 1\n\t" \
  "ds_write_b32 v80, v81\n\t" \
  "s_waitcnt lgkmcnt(0)\n\t" \
  ".Laok%=:\n\t" \
  "s_barrier\n\t" \
  "ds_read_b32 v81, v80\n\t" \
  "s_waitcnt lgkmcnt(0)\n\t" \
  "v_readfirstlane_b32 s63, v81\n\t" \
  "s_nop 1\n\t" \
  "s_cmp_gt_u32 s63, s42\n\t" \
  "s_cbranch_scc1 .Lafall%=\n\t" \
  "s_or_b32 s63, s35, s32\n\t" \
  "s_cbranch_scc1 .Lanoe%=\n\t" \
  "v_mov_b64 v[0:1], s[28:29]\n\t" \
  "v_mov_b32 v2, 0\n\t" \
  "s_mov_b64 exec, 1\n\t" \
  "global_store_dwordx2 v2, v[0:1], s[24:25] sc1\n\t" \
  "s_mov_b64 exec, -1\n\t" \
  ".Lanoe%=:\n\t" \
  "s_mov_b32 exec_hi, 0xff\n\t" \
  "v_lshrrev_b32 v28, 4, v237\n\t" \
  "v_mul_u32_u24 v29, 205, v28\n\t" \
  "v_lshrrev_b32 v29, 11, v29\n\t" \
  "v_mul_u32_u24 v30, 10, v29\n\t" \
  "v_sub_u32 v28, v28, v30\n\t" \
  "v_lshl_add_u32 v29, s32, 2, v29\n\t" \
  "s_lshr_b32 s63, s62, 3\n\t" \
  "s_mul_i32 s63, s63, 81920\n\t" \
  "s_and_b32 s64, s62, 7\n\t" \
  "s_mul_i32 s64, s64, 160\n\t" \
  "s_add_u32 s63, s63, s64\n\t" \
  "v_mul_u32_u24 v30, 0x500, v29\n\t" \
  "v_lshl_add_u32 v20, v28, 4, s63\n\t" \
  "v_add_u32 v20, v20, v30\n\t" \
  "v_add_u32 v21, 20480, v20\n\t" \
  "v_add_u32 v22, 40960, v20\n\t" \
  "v_add_u32 v23, 61440, v20\n\t" \
  "global_load_dwordx4 v[96:99], v20, s[8:9]\n\t" \
  "global_load_dwordx4 v[100:103], v21, s[8:9]\n\t" \
  "global_load_dwordx4 v[104:107], v22, s[8:9]\n\t" \
  "global_load_dwordx4 v[108:111], v23, s[8:9]\n\t" \
  "v_mul_u32_u24 v30, 336, v29\n\t" \
  "v_lshl_add_u32 v26, v28, 5, v30\n\t" \
  "v_add_u32 v27, 64512, v26\n\t" \
  "v_add_u32 v26, 21504, v26\n\t" \
  "s_waitcnt vmcnt(4)\n\t" \
  "s_barrier\n\t" \
  "s_waitcnt vmcnt(0)\n\t" \
  "v_lshlrev_b32 v32, 16, v96\n\t" \
  "v_and_b32 v33, 0xffff0000, v96\n\t" \
  "v_lshlrev_b32 v34, 16, v97\n\t" \
  "v_and_b32 v35, 0xffff0000, v97\n\t" \
  "v_lshlrev_b32 v36, 16, v98\n\t" \
  "v_and_b32 v37, 0xffff0000, v98\n\t" \
  "v_lshlrev_b32 v38, 16, v99\n\t" \
  "v_and_b32 v39, 0xffff0000, v99\n\t" \
  "v_lshlrev_b32 v40, 16, v100\n\t" \
  "v_and_b32 v41, 0xffff0000, v100\n\t" \
  "v_lshlrev_b32 v42, 16, v101\n\t" \
  "v_and_b32 v43, 0xffff0000, v101\n\t" \
  "v_lshlrev_b32 v44, 16, v102\n\t" \
  "v_and_b32 v45, 0xffff0000, v102\n\t" \
  "v_lshlrev_b32 v46, 16, v103\n\t" \
  "v_and_b32 v47, 0xffff0000, v103\n\t" \
  "v_lshlrev_b32 v48, 16, v104\n\t" \
  "v_and_b32 v49, 0xffff0000, v104\n\t" \
  "v_lshlrev_b32 v50, 16, v105\n\t" \
  "v_and_b32 v51, 0xffff0000, v105\n\t" \
  "v_lshlrev_b32 v52, 16, v106\n\t" \
  "v_and_b32 v53, 0xffff0000, v106\n\t" \
  "v_lshlrev_b32 v54, 16, v107\n\t" \
  "v_and_b32 v55, 0xffff0000, v107\n\t" \
  "v_lshlrev_b32 v56, 16, v108\n\t" \
  "v_and_b32 v57, 0xffff0000, v108\n\t" \
  "v_lshlrev_b32 v58, 16, v109\n\t" \
  "v_and_b32 v59, 0xffff0000, v109\n\t" \
  "v_lshlrev_b32 v60, 16, v110\n\t" \
  "v_and_b32 v61, 0xffff0000, v110\n\t" \
  "v_lshlrev_b32 v62, 16, v111\n\t" \
  "v_and_b32 v63, 0xffff0000, v111\n\t" \
  "ds_read_b128 v[64:67], v26 offset:0\n\t" \
  "ds_read_b128 v[68:71], v26 offset:16\n\t" \
  "ds_read_b128 v[72:75], v26 offset:21504\n\t" \
  "ds_read_b128 v[76:79], v26 offset:21520\n\t" \
  "ds_read_b128 v[80:83], v27 offset:0\n\t" \
  "ds_read_b128 v[84:87], v27 offset:16\n\t" \
  "ds_read_b128 v[88:91], v27 offset:21504\n\t" \
  "ds_read_b128 v[92:95], v27 offset:21520\n\t" \
  "s_waitcnt lgkmcnt(6)\n\t" \
  "v_add_f32 v32, v32, v64\n\t" \
  "v_add_f32 v33, v33, v65\n\t" \
  "v_add_f32 v34, v34, v66\n\t" \
  "v_add_f32 v35, v35, v67\n\t" \
  "v_add_f32 v36, v36, v68\n\t" \
  "v_add_f32 v37, v37, v69\n\t" \
  "v_add_f32 v38, v38, v70\n\t" \
  "v_add_f32 v39, v39, v71\n\t" \
  "s_waitcnt lgkmcnt(4)\n\t" \
  "v_add_f32 v32, v32, v72\n\t" \
  "v_add_f32 v33, v33, v73\n\t" \
  "v_add_f32 v34, v34, v74\n\t" \
  "v_add_f32 v35, v35, v75\n\t" \
  "v_add_f32 v36, v36, v76\n\t" \
  "v_add_f32 v37, v37, v77\n\t" \
  "v_add_f32 v38, v38, v78\n\t" \
  "v_add_f32 v39, v39, v79\n\t" \
  "s_waitcnt lgkmcnt(2)\n\t" \
  "v_add_f32 v32, v32, v80\n\t" \
  "v_add_f32 v33, v33, v81\n\t" \
  "v_add_f32 v34, v34, v82\n\t" \
  "v_add_f32 v35, v35, v83\n\t" \
  "v_add_f32 v36, v36, v84\n\t" \
  "v_add_f32 v37, v37, v85\n\t" \
  "v_add_f32 v38, v38, v86\n\t" \
  "v_add_f32 v39, v39, v87\n\t" \
  "s_waitcnt lgkmcnt(0)\n\t" \
  "v_add_f32 v32, v32, v88\n\t" \
  "v_add_f32 v33, v33, v89\n\t" \
  "v_add_f32 v34, v34, v90\n\t" \
  "v_add_f32 v35, v35, v91\n\t" \
  "v_add_f32 v36, v36, v92\n\t" \
  "v_add_f32 v37, v37, v93\n\t" \
  "v_add_f32 v38, v38, v94\n\t" \
  "v_add_f32 v39, v39, v95\n\t" \
  "v_cvt_pk_bf16_f32 v96, v32, v33\n\t" \
  "v_cvt_pk_bf16_f32 v97, v34, v35\n\t" \
  "v_cvt_pk_bf16_f32 v98, v36, v37\n\t" \
  "v_cvt_pk_bf16_f32 v99, v38, v39\n\t" \
  "global_store_dwordx4 v20, v[96:99], s[26:27] nt\n\t" \
  "s_barrier\n\t" \
  "ds_read_b128 v[64:67], v26 offset:5376\n\t" \
  "ds_read_b128 v[68:71], v26 offset:5392\n\t" \
  "ds_read_b128 v[72:75], v26 offset:26880\n\t" \
  "ds_read_b128 v[76:79], v26 offset:26896\n\t" \
  "ds_read_b128 v[80:83], v27 offset:5376\n\t" \
  "ds_read_b128 v[84:87], v27 offset:5392\n\t" \
  "ds_read_b128 v[88:91], v27 offset:26880\n\t" \
  "ds_read_b128 v[92:95], v27 offset:26896\n\t" \
  "s_waitcnt lgkmcnt(6)\n\t" \
  "v_add_f32 v40, v40, v64\n\t" \
  "v_add_f32 v41, v41, v65\n\t" \
  "v_add_f32 v42, v42, v66\n\t" \
  "v_add_f32 v43, v43, v67\n\t" \
  "v_add_f32 v44, v44, v68\n\t" \
  "v_add_f32 v45, v45, v69\n\t" \
  "v_add_f32 v46, v46, v70\n\t" \
  "v_add_f32 v47, v47, v71\n\t" \
  "s_waitcnt lgkmcnt(4)\n\t" \
  "v_add_f32 v40, v40, v72\n\t" \
  "v_add_f32 v41, v41, v73\n\t" \
  "v_add_f32 v42, v42, v74\n\t" \
  "v_add_f32 v43, v43, v75\n\t" \
  "v_add_f32 v44, v44, v76\n\t" \
  "v_add_f32 v45, v45, v77\n\t" \
  "v_add_f32 v46, v46, v78\n\t" \
  "v_add_f32 v47, v47, v79\n\t" \
  "s_waitcnt lgkmcnt(2)\n\t" \
  "v_add_f32 v40, v40, v80\n\t" \
  "v_add_f32 v41, v41, v81\n\t" \
  "v_add_f32 v42, v42, v82\n\t" \
  "v_add_f32 v43, v43, v83\n\t" \
  "v_add_f32 v44, v44, v84\n\t" \
  "v_add_f32 v45, v45, v85\n\t" \
  "v_add_f32 v46, v46, v86\n\t" \
  "v_add_f32 v47, v47, v87\n\t" \
  "s_waitcnt lgkmcnt(0)\n\t" \
  "v_add_f32 v40, v40, v88\n\t" \
  "v_add_f32 v41, v41, v89\n\t" \
  "v_add_f32 v42, v42, v90\n\t" \
  "v_add_f32 v43, v43, v91\n\t" \
  "v_add_f32 v44, v44, v92\n\t" \
  "v_add_f32 v45, v45, v93\n\t" \
  "v_add_f32 v46, v46, v94\n\t" \
  "v_add_f32 v47, v47, v95\n\t" \
  "v_cvt_pk_bf16_f32 v100, v40, v41\n\t" \
  "v_cvt_pk_bf16_f32 v101, v42, v43\n\t" \
  "v_cvt_pk_bf16_f32 v102, v44, v45\n\t" \
  "v_cvt_pk_bf16_f32 v103, v46, v47\n\t" \
  "global_store_dwordx4 v21, v[100:103], s[26:27] nt\n\t" \
  "s_barrier\n\t" \
  "ds_read_b128 v[64:67], v26 offset:10752\n\t" \
  "ds_read_b128 v[68:71], v26 offset:10768\n\t" \
  "ds_read_b128 v[72:75], v26 offset:32256\n\t" \
  "ds_read_b128 v[76:79], v26 offset:32272\n\t" \
  "ds_read_b128 v[80:83], v27 offset:10752\n\t" \
  "ds_read_b128 v[84:87], v27 offset:10768\n\t" \
  "ds_read_b128 v[88:91], v27 offset:32256\n\t" \
  "ds_read_b128 v[92:95], v27 offset:32272\n\t" \
  "s_waitcnt lgkmcnt(6)\n\t" \
  "v_add_f32 v48, v48, v64\n\t" \
  "v_add_f32 v49, v49, v65\n\t" \
  "v_add_f32 v50, v50, v66\n\t" \
  "v_add_f32 v51, v51, v67\n\t" \
  "v_add_f32 v52, v52, v68\n\t" \
  "v_add_f32 v53, v53, v69\n\t" \
  "v_add_f32 v54, v54, v70\n\t" \
  "v_add_f32 v55, v55, v71\n\t" \
  "s_waitcnt lgkmcnt(4)\n\t" \
  "v_add_f32 v48, v48, v72\n\t" \
  "v_add_f32 v49, v49, v73\n\t" \
  "v_add_f32 v50, v50, v74\n\t" \
  "v_add_f32 v51, v51, v75\n\t" \
  "v_add_f32 v52, v52, v76\n\t" \
  "v_add_f32 v53, v53, v77\n\t" \
  "v_add_f32 v54, v54, v78\n\t" \
  "v_add_f32 v55, v55, v79\n\t" \
  "s_waitcnt lgkmcnt(2)\n\t" \
  "v_add_f32 v48, v48, v80\n\t" \
  "v_add_f32 v49, v49, v81\n\t" \
  "v_add_f32 v50, v50, v82\n\t" \
  "v_add_f32 v51, v51, v83\n\t" \
  "v_add_f32 v52, v52, v84\n\t" \
  "v_add_f32 v53, v53, v85\n\t" \
  "v_add_f32 v54, v54, v86\n\t" \
  "v_add_f32 v55, v55, v87\n\t" \
  "s_waitcnt lgkmcnt(0)\n\t" \
  "v_add_f32 v48, v48, v88\n\t" \
  "v_add_f32 v49, v49, v89\n\t" \
  "v_add_f32 v50, v50, v90\n\t" \
  "v_add_f32 v51, v51, v91\n\t" \
  "v_add_f32 v52, v52, v92\n\t" \
  "v_add_f32 v53, v53, v93\n\t" \
  "v_add_f32 v54, v54, v94\n\t" \
  "v_add_f32 v55, v55, v95\n\t" \
  "v_cvt_pk_bf16_f32 v104, v48, v49\n\t" \
  "v_cvt_pk_bf16_f32 v105, v50, v51\n\t" \
  "v_cvt_pk_bf16_f32 v106, v52, v53\n\t" \
  "v_cvt_pk_bf16_f32 v107, v54, v55\n\t" \
  "global_store_dwordx4 v22, v[104:107], s[26:27] nt\n\t" \
  "s_barrier\n\t" \
  "ds_read_b128 v[64:67], v26 offset:16128\n\t" \
  "ds_read_b128 v[68:71], v26 offset:16144\n\t" \
  "ds_read_b128 v[72:75], v26 offset:37632\n\t" \
  "ds_read_b128 v[76:79], v26 offset:37648\n\t" \
  "ds_read_b128 v[80:83], v27 offset:16128\n\t" \
  "ds_read_b128 v[84:87], v27 offset:16144\n\t" \
  "ds_read_b128 v[88:91], v27 offset:37632\n\t" \
  "ds_read_b128 v[92:95], v27 offset:37648\n\t" \
  "s_waitcnt lgkmcnt(6)\n\t" \
  "v_pk_add_f32 v[56:57], v[56:57], v[64:65]\n\t" \
  "v_pk_add_f32 v[58:59], v[58:59], v[66:67]\n\t" \
  "v_pk_add_f32 v[60:61], v[60:61], v[68:69]\n\t" \
  "v_pk_add_f32 v[62:63], v[62:63], v[70:71]\n\t" \
  "s_waitcnt lgkmcnt(4)\n\t" \
  "v_pk_add_f32 v[56:57], v[56:57], v[72:73]\n\t" \
  "v_pk_add_f32 v[58:59], v[58:59], v[74:75]\n\t" \
  "v_pk_add_f32 v[60:61], v[60:61], v[76:77]\n\t" \
  "v_pk_add_f32 v[62:63], v[62:63], v[78:79]\n\t" \
  "s_waitcnt lgkmcnt(2)\n\t" \
  "v_pk_add_f32 v[56:57], v[56:57], v[80:81]\n\t" \
  "v_pk_add_f32 v[58:59], v[58:59], v[82:83]\n\t" \
  "v_pk_add_f32 v[60:61], v[60:61], v[84:85]\n\t" \
  "v_pk_add_f32 v[62:63], v[62:63], v[86:87]\n\t" \
  "s_waitcnt lgkmcnt(0)\n\t" \
  "v_pk_add_f32 v[56:57], v[56:57], v[88:89]\n\t" \
  "v_pk_add_f32 v[58:59], v[58:59], v[90:91]\n\t" \
  "v_pk_add_f32 v[60:61], v[60:61], v[92:93]\n\t" \
  "v_pk_add_f32 v[62:63], v[62:63], v[94:95]\n\t" \
  "v_cvt_pk_bf16_f32 v108, v56, v57\n\t" \
  "v_cvt_pk_bf16_f32 v109, v58, v59\n\t" \
  "v_cvt_pk_bf16_f32 v110, v60, v61\n\t" \
  "v_cvt_pk_bf16_f32 v111, v62, v63\n\t" \
  "global_store_dwordx4 v23, v[108:111], s[26:27] nt\n\t" \
  "s_mov_b32 exec_hi, -1\n\t" \
  "s_branch .Lend%=\n\t" \
  ".Lafall%=:\n\t" \
  "s_mov_b32 s42, 1\n\t" \
  "s_mov_b32 s43, -1\n\t" \
  "s_mov_b64 s[40:41], s[36:37]\n\t" \
  "s_mov_b32 s63, 15\n\t" \
  "v_mov_b32 v0, 0xff800000\n\t" \
  "v_mov_b32 v1, 0xff800000\n\t" \
  "v_mov_b32 v2, 0xff800000\n\t" \
  "v_mov_b32 v3, 0xff800000\n\t" \
  "v_mov_b32 v4, 0xff800000\n\t" \
  "v_mov_b32 v5, 0xff800000\n\t" \
  "v_mov_b32 v6, 0xff800000\n\t" \
  "v_mov_b32 v7, 0xff800000\n\t" \
  ".Lamax%=:\n\t" \
  "global_load_dwordx4 v[208:211], v237, s[40:41] offset:0 sc1\n\t" \
  "global_load_dwordx2 v[216:217], v238, s[40:41] offset:3072 sc1\n\t" \
  "s_add_u32 s40, s40, 3584\n\t" \
  "s_addc_u32 s41, s41, 0\n\t" \
  "s_waitcnt vmcnt(0)\n\t" \
  "v_mfma_f32_32x32x16_bf16 v[128:143], v[208:211], v[160:163], 0\n\t" \
  "v_mfma_f32_32x32x8_bf16 v[128:143], v[216:217], v[192:193], v[128:143]\n\t" \
  "s_nop 11\n\t" \
  "v_max3_f32 v0, v0, v128, v129\n\t" \
  "v_max3_f32 v0, v0, v130, v131\n\t" \
  "v_max3_f32 v0, v0, v132, v133\n\t" \
  "v_max3_f32 v0, v0, v134, v135\n\t" \
  "v_max3_f32 v0, v0, v136, v137\n\t" \
  "v_max3_f32 v0, v0, v138, v139\n\t" \
  "v_max3_f32 v0, v0, v140, v141\n\t" \
  "v_max3_f32 v0, v0, v142, v143\n\t" \
  "v_mfma_f32_32x32x16_bf16 v[128:143], v[208:211], v[164:167], 0\n\t" \
  "v_mfma_f32_32x32x8_bf16 v[128:143], v[216:217], v[194:195], v[128:143]\n\t" \
  "s_nop 11\n\t" \
  "v_max3_f32 v1, v1, v128, v129\n\t" \
  "v_max3_f32 v1, v1, v130, v131\n\t" \
  "v_max3_f32 v1, v1, v132, v133\n\t" \
  "v_max3_f32 v1, v1, v134, v135\n\t" \
  "v_max3_f32 v1, v1, v136, v137\n\t" \
  "v_max3_f32 v1, v1, v138, v139\n\t" \
  "v_max3_f32 v1, v1, v140, v141\n\t" \
  "v_max3_f32 v1, v1, v142, v143\n\t" \
  "v_mfma_f32_32x32x16_bf16 v[128:143], v[208:211], v[168:171], 0\n\t" \
  "v_mfma_f32_32x32x8_bf16 v[128:143], v[216:217], v[196:197], v[128:143]\n\t" \
  "s_nop 11\n\t" \
  "v_max3_f32 v2, v2, v128, v129\n\t" \
  "v_max3_f32 v2, v2, v130, v131\n\t" \
  "v_max3_f32 v2, v2, v132, v133\n\t" \
  "v_max3_f32 v2, v2, v134, v135\n\t" \
  "v_max3_f32 v2, v2, v136, v137\n\t" \
  "v_max3_f32 v2, v2, v138, v139\n\t" \
  "v_max3_f32 v2, v2, v140, v141\n\t" \
  "v_max3_f32 v2, v2, v142, v143\n\t" \
  "v_mfma_f32_32x32x16_bf16 v[128:143], v[208:211], v[172:175], 0\n\t" \
  "v_mfma_f32_32x32x8_bf16 v[128:143], v[216:217], v[198:199], v[128:143]\n\t" \
  "s_nop 11\n\t" \
  "v_max3_f32 v3, v3, v128, v129\n\t" \
  "v_max3_f32 v3, v3, v130, v131\n\t" \
  "v_max3_f32 v3, v3, v132, v133\n\t" \
  "v_max3_f32 v3, v3, v134, v135\n\t" \
  "v_max3_f32 v3, v3, v136, v137\n\t" \
  "v_max3_f32 v3, v3, v138, v139\n\t" \
  "v_max3_f32 v3, v3, v140, v141\n\t" \
  "v_max3_f32 v3, v3, v142, v143\n\t" \
  "v_mfma_f32_32x32x16_bf16 v[128:143], v[208:211], v[176:179], 0\n\t" \
  "v_mfma_f32_32x32x8_bf16 v[128:143], v[216:217], v[200:201], v[128:143]\n\t" \
  "s_nop 11\n\t" \
  "v_max3_f32 v4, v4, v128, v129\n\t" \
  "v_max3_f32 v4, v4, v130, v131\n\t" \
  "v_max3_f32 v4, v4, v132, v133\n\t" \
  "v_max3_f32 v4, v4, v134, v135\n\t" \
  "v_max3_f32 v4, v4, v136, v137\n\t" \
  "v_max3_f32 v4, v4, v138, v139\n\t" \
  "v_max3_f32 v4, v4, v140, v141\n\t" \
  "v_max3_f32 v4, v4, v142, v143\n\t" \
  "v_mfma_f32_32x32x16_bf16 v[128:143], v[208:211], v[180:183], 0\n\t" \
  "v_mfma_f32_32x32x8_bf16 v[128:143], v[216:217], v[202:203], v[128:143]\n\t" \
  "s_nop 11\n\t" \
  "v_max3_f32 v5, v5, v128, v129\n\t" \
  "v_max3_f32 v5, v5, v130, v131\n\t" \
  "v_max3_f32 v5, v5, v132, v133\n\t" \
  "v_max3_f32 v5, v5, v134, v135\n\t" \
  "v_max3_f32 v5, v5, v136, v137\n\t" \
  "v_max3_f32 v5, v5, v138, v139\n\t" \
  "v_max3_f32 v5, v5, v140, v141\n\t" \
  "v_max3_f32 v5, v5, v142, v143\n\t" \
  "v_mfma_f32_32x32x16_bf16 v[128:143], v[208:211], v[184:187], 0\n\t" \
  "v_mfma_f32_32x32x8_bf16 v[128:143], v[216:217], v[204:205], v[128:143]\n\t" \
  "s_nop 11\n\t" \
  "v_max3_f32 v6, v6, v128, v129\n\t" \
  "v_max3_f32 v6, v6, v130, v131\n\t" \
  "v_max3_f32 v6, v6, v132, v133\n\t" \
  "v_max3_f32 v6, v6, v134, v135\n\t" \
  "v_max3_f32 v6, v6, v136, v137\n\t" \
  "v_max3_f32 v6, v6, v138, v139\n\t" \
  "v_max3_f32 v6, v6, v140, v141\n\t" \
  "v_max3_f32 v6, v6, v142, v143\n\t" \
  "v_mfma_f32_32x32x16_bf16 v[128:143], v[208:211], v[188:191], 0\n\t" \
  "v_mfma_f32_32x32x8_bf16 v[128:143], v[216:217], v[206:207], v[128:143]\n\t" \
  "s_nop 11\n\t" \
  "v_max3_f32 v7, v7, v128, v129\n\t" \
  "v_max3_f32 v7, v7, v130, v131\n\t" \
  "v_max3_f32 v7, v7, v132, v133\n\t" \
  "v_max3_f32 v7, v7, v134, v135\n\t" \
  "v_max3_f32 v7, v7, v136, v137\n\t" \
  "v_max3_f32 v7, v7, v138, v139\n\t" \
  "v_max3_f32 v7, v7, v140, v141\n\t" \
  "v_max3_f32 v7, v7, v142, v143\n\t" \
  "s_sub_u32 s63, s63, 1\n\t" \
  "s_cbranch_scc0 .Lamax%=\n\t" \
  "s_lshl_b32 s63, s32, 8\n\t" \
  "v_lshrrev_b32 v8, 2, v237\n\t" \
  "v_and_b32 v9, 0x7c, v8\n\t" \
  "v_add_u32 v8, s63, v8\n\t" \
  "ds_write_b32 v8, v0 offset:21504\n\t" \
  "ds_write_b32 v8, v1 offset:22528\n\t" \
  "ds_write_b32 v8, v2 offset:23552\n\t" \
  "ds_write_b32 v8, v3 offset:24576\n\t" \
  "ds_write_b32 v8, v4 offset:25600\n\t" \
  "ds_write_b32 v8, v5 offset:26624\n\t" \
  "ds_write_b32 v8, v6 offset:27648\n\t" \
  "ds_write_b32 v8, v7 offset:28672\n\t" \
  "s_waitcnt lgkmcnt(0)\n\t" \
  "s_barrier\n\t" \
  "ds_read_b32 v16, v9 offset:21504\n\t" \
  "ds_read_b32 v17, v9 offset:21632\n\t" \
  "ds_read_b32 v18, v9 offset:21760\n\t" \
  "ds_read_b32 v19, v9 offset:21888\n\t" \
  "ds_read_b32 v20, v9 offset:22016\n\t" \
  "ds_read_b32 v21, v9 offset:22144\n\t" \
  "ds_read_b32 v22, v9 offset:22272\n\t" \
  "ds_read_b32 v23, v9 offset:22400\n\t" \
  "s_waitcnt lgkmcnt(0)\n\t" \
  "v_max3_f32 v16, v16, v17, v18\n\t" \
  "v_max3_f32 v16, v16, v19, v20\n\t" \
  "v_max3_f32 v16, v16, v21, v22\n\t" \
  "v_max_f32 v16, v16, v23\n\t" \
  "v_sub_f32 v16, 0, v16\n\t" \
  "v_and_b32 v17, 0xffff0000, v16\n\t" \
  "v_sub_f32 v16, v16, v17\n\t" \
  "v_and_b32 v18, 0xffff0000, v16\n\t" \
  "v_sub_f32 v16, v16, v18\n\t" \
  "v_lshrrev_b32 v17, 16, v17\n\t" \
  "s_mov_b32 exec_lo, 0\n\t" \
  "v_or_b32 v192, v17, v18\n\t" \
  "v_lshrrev_b32 v193, 16, v16\n\t" \
  "s_mov_b32 exec_lo, -1\n\t" \
  "ds_read_b32 v16, v9 offset:22528\n\t" \
  "ds_read_b32 v17, v9 offset:22656\n\t" \
  "ds_read_b32 v18, v9 offset:22784\n\t" \
  "ds_read_b32 v19, v9 offset:22912\n\t" \
  "ds_read_b32 v20, v9 offset:23040\n\t" \
  "ds_read_b32 v21, v9 offset:23168\n\t" \
  "ds_read_b32 v22, v9 offset:23296\n\t" \
  "ds_read_b32 v23, v9 offset:23424\n\t" \
  "s_waitcnt lgkmcnt(0)\n\t" \
  "v_max3_f32 v16, v16, v17, v18\n\t" \
  "v_max3_f32 v16, v16, v19, v20\n\t" \
  "v_max3_f32 v16, v16, v21, v22\n\t" \
  "v_max_f32 v16, v16, v23\n\t" \
  "v_sub_f32 v16, 0, v16\n\t" \
  "v_and_b32 v17, 0xffff0000, v16\n\t" \
  "v_sub_f32 v16, v16, v17\n\t" \
  "v_and_b32 v18, 0xffff0000, v16\n\t" \
  "v_sub_f32 v16, v16, v18\n\t" \
  "v_lshrrev_b32 v17, 16, v17\n\t" \
  "s_mov_b32 exec_lo, 0\n\t" \
  "v_or_b32 v194, v17, v18\n\t" \
  "v_lshrrev_b32 v195, 16, v16\n\t" \
  "s_mov_b32 exec_lo, -1\n\t" \
  "ds_read_b32 v16, v9 offset:23552\n\t" \
  "ds_read_b32 v17, v9 offset:23680\n\t" \
  "ds_read_b32 v18, v9 offset:23808\n\t" \
  "ds_read_b32 v19, v9 offset:23936\n\t" \
  "ds_read_b32 v20, v9 offset:24064\n\t" \
  "ds_read_b32 v21, v9 offset:24192\n\t" \
  "ds_read_b32 v22, v9 offset:24320\n\t" \
  "ds_read_b32 v23, v9 offset:24448\n\t" \
  "s_waitcnt lgkmcnt(0)\n\t" \
  "v_max3_f32 v16, v16, v17, v18\n\t" \
  "v_max3_f32 v16, v16, v19, v20\n\t" \
  "v_max3_f32 v16, v16, v21, v22\n\t" \
  "v_max_f32 v16, v16, v23\n\t" \
  "v_sub_f32 v16, 0, v16\n\t" \
  "v_and_b32 v17, 0xffff0000, v16\n\t" \
  "v_sub_f32 v16, v16, v17\n\t" \
  "v_and_b32 v18, 0xffff0000, v16\n\t" \
  "v_sub_f32 v16, v16, v18\n\t" \
  "v_lshrrev_b32 v17, 16, v17\n\t" \
  "s_mov_b32 exec_lo, 0\n\t" \
  "v_or_b32 v196, v17, v18\n\t" \
  "v_lshrrev_b32 v197, 16, v16\n\t" \
  "s_mov_b32 exec_lo, -1\n\t" \
  "ds_read_b32 v16, v9 offset:24576\n\t" \
  "ds_read_b32 v17, v9 offset:24704\n\t" \
  "ds_read_b32 v18, v9 offset:24832\n\t" \
  "ds_read_b32 v19, v9 offset:24960\n\t" \
  "ds_read_b32 v20, v9 offset:25088\n\t" \
  "ds_read_b32 v21, v9 offset:25216\n\t" \
  "ds_read_b32 v22, v9 offset:25344\n\t" \
  "ds_read_b32 v23, v9 offset:25472\n\t" \
  "s_waitcnt lgkmcnt(0)\n\t" \
  "v_max3_f32 v16, v16, v17, v18\n\t" \
  "v_max3_f32 v16, v16, v19, v20\n\t" \
  "v_max3_f32 v16, v16, v21, v22\n\t" \
  "v_max_f32 v16, v16, v23\n\t" \
  "v_sub_f32 v16, 0, v16\n\t" \
  "v_and_b32 v17, 0xffff0000, v16\n\t" \
  "v_sub_f32 v16, v16, v17\n\t" \
  "v_and_b32 v18, 0xffff0000, v16\n\t" \
  "v_sub_f32 v16, v16, v18\n\t" \
  "v_lshrrev_b32 v17, 16, v17\n\t" \
  "s_mov_b32 exec_lo, 0\n\t" \
  "v_or_b32 v198, v17, v18\n\t" \
  "v_lshrrev_b32 v199, 16, v16\n\t" \
  "s_mov_b32 exec_lo, -1\n\t" \
  "ds_read_b32 v16, v9 offset:25600\n\t" \
  "ds_read_b32 v17, v9 offset:25728\n\t" \
  "ds_read_b32 v18, v9 offset:25856\n\t" \
  "ds_read_b32 v19, v9 offset:25984\n\t" \
  "ds_read_b32 v20, v9 offset:26112\n\t" \
  "ds_read_b32 v21, v9 offset:26240\n\t" \
  "ds_read_b32 v22, v9 offset:26368\n\t" \
  "ds_read_b32 v23, v9 offset:26496\n\t" \
  "s_waitcnt lgkmcnt(0)\n\t" \
  "v_max3_f32 v16, v16, v17, v18\n\t" \
  "v_max3_f32 v16, v16, v19, v20\n\t" \
  "v_max3_f32 v16, v16, v21, v22\n\t" \
  "v_max_f32 v16, v16, v23\n\t" \
  "v_sub_f32 v16, 0, v16\n\t" \
  "v_and_b32 v17, 0xffff0000, v16\n\t" \
  "v_sub_f32 v16, v16, v17\n\t" \
  "v_and_b32 v18, 0xffff0000, v16\n\t" \
  "v_sub_f32 v16, v16, v18\n\t" \
  "v_lshrrev_b32 v17, 16, v17\n\t" \
  "s_mov_b32 exec_lo, 0\n\t" \
  "v_or_b32 v200, v17, v18\n\t" \
  "v_lshrrev_b32 v201, 16, v16\n\t" \
  "s_mov_b32 exec_lo, -1\n\t" \
  "ds_read_b32 v16, v9 offset:26624\n\t" \
  "ds_read_b32 v17, v9 offset:26752\n\t" \
  "ds_read_b32 v18, v9 offset:26880\n\t" \
  "ds_read_b32 v19, v9 offset:27008\n\t" \
  "ds_read_b32 v20, v9 offset:27136\n\t" \
  "ds_read_b32 v21, v9 offset:27264\n\t" \
  "ds_read_b32 v22, v9 offset:27392\n\t" \
  "ds_read_b32 v23, v9 offset:27520\n\t" \
  "s_waitcnt lgkmcnt(0)\n\t" \
  "v_max3_f32 v16, v16, v17, v18\n\t" \
  "v_max3_f32 v16, v16, v19, v20\n\t" \
  "v_max3_f32 v16, v16, v21, v22\n\t" \
  "v_max_f32 v16, v16, v23\n\t" \
  "v_sub_f32 v16, 0, v16\n\t" \
  "v_and_b32 v17, 0xffff0000, v16\n\t" \
  "v_sub_f32 v16, v16, v17\n\t" \
  "v_and_b32 v18, 0xffff0000, v16\n\t" \
  "v_sub_f32 v16, v16, v18\n\t" \
  "v_lshrrev_b32 v17, 16, v17\n\t" \
  "s_mov_b32 exec_lo, 0\n\t" \
  "v_or_b32 v202, v17, v18\n\t" \
  "v_lshrrev_b32 v203, 16, v16\n\t" \
  "s_mov_b32 exec_lo, -1\n\t" \
  "ds_read_b32 v16, v9 offset:27648\n\t" \
  "ds_read_b32 v17, v9 offset:27776\n\t" \
  "ds_read_b32 v18, v9 offset:27904\n\t" \
  "ds_read_b32 v19, v9 offset:28032\n\t" \
  "ds_read_b32 v20, v9 offset:28160\n\t" \
  "ds_read_b32 v21, v9 offset:28288\n\t" \
  "ds_read_b32 v22, v9 offset:28416\n\t" \
  "ds_read_b32 v23, v9 offset:28544\n\t" \
  "s_waitcnt lgkmcnt(0)\n\t" \
  "v_max3_f32 v16, v16, v17, v18\n\t" \
  "v_max3_f32 v16, v16, v19, v20\n\t" \
  "v_max3_f32 v16, v16, v21, v22\n\t" \
  "v_max_f32 v16, v16, v23\n\t" \
  "v_sub_f32 v16, 0, v16\n\t" \
  "v_and_b32 v17, 0xffff0000, v16\n\t" \
  "v_sub_f32 v16, v16, v17\n\t" \
  "v_and_b32 v18, 0xffff0000, v16\n\t" \
  "v_sub_f32 v16, v16, v18\n\t" \
  "v_lshrrev_b32 v17, 16, v17\n\t" \
  "s_mov_b32 exec_lo, 0\n\t" \
  "v_or_b32 v204, v17, v18\n\t" \
  "v_lshrrev_b32 v205, 16, v16\n\t" \
  "s_mov_b32 exec_lo, -1\n\t" \
  "ds_read_b32 v16, v9 offset:28672\n\t" \
  "ds_read_b32 v17, v9 offset:28800\n\t" \
  "ds_read_b32 v18, v9 offset:28928\n\t" \
  "ds_read_b32 v19, v9 offset:29056\n\t" \
  "ds_read_b32 v20, v9 offset:29184\n\t" \
  "ds_read_b32 v21, v9 offset:29312\n\t" \
  "ds_read_b32 v22, v9 offset:29440\n\t" \
  "ds_read_b32 v23, v9 offset:29568\n\t" \
  "s_waitcnt lgkmcnt(0)\n\t" \
  "v_max3_f32 v16, v16, v17, v18\n\t" \
  "v_max3_f32 v16, v16, v19, v20\n\t" \
  "v_max3_f32 v16, v16, v21, v22\n\t" \
  "v_max_f32 v16, v16, v23\n\t" \
  "v_sub_f32 v16, 0, v16\n\t" \
  "v_and_b32 v17, 0xffff0000, v16\n\t" \
  "v_sub_f32 v16, v16, v17\n\t" \
  "v_and_b32 v18, 0xffff0000, v16\n\t" \
  "v_sub_f32 v16, v16, v18\n\t" \
  "v_lshrrev_b32 v17, 16, v17\n\t" \
  "s_mov_b32 exec_lo, 0\n\t" \
  "v_or_b32 v206, v17, v18\n\t" \
  "v_lshrrev_b32 v207, 16, v16\n\t" \
  "s_mov_b32 exec_lo, -1\n\t" \
  "global_load_dwordx4 v[208:211], v237, s[36:37] offset:0 sc1\n\t" \
  "global_load_dwordx2 v[216:217], v238, s[36:37] offset:3072 sc1\n\t" \
  "global_load_dwordx4 v[220:223], v237, s[36:37] offset:1024 sc1\n\t" \
  "global_load_dwordx4 v[224:227], v237, s[36:37] offset:2048 sc1\n\t" \
  "s_barrier\n\t" \
  "s_branch .Lago%=\n\t" \
  ".Lvarq%=:\n\t" \
  "v_lshlrev_b32 v36, 16, v10\n\t" \
  "v_and_b32 v37, 0xffff0000, v10\n\t" \
  "v_lshlrev_b32 v38, 16, v11\n\t" \
  "v_and_b32 v39, 0xffff0000, v11\n\t" \
  "v_lshlrev_b32 v40, 16, v12\n\t" \
  "v_and_b32 v41, 0xffff0000, v12\n\t" \
  "v_lshlrev_b32 v42, 16, v13\n\t" \
  "v_and_b32 v43, 0xffff0000, v13\n\t" \
  "v_lshlrev_b32 v44, 16, v14\n\t" \
  "v_and_b32 v45, 0xffff0000, v14\n\t" \
  "v_sub_f32 v36, v36, v48\n\t" \
  "v_sub_f32 v37, v37, v48\n\t" \
  "v_sub_f32 v38, v38, v48\n\t" \
  "v_sub_f32 v39, v39, v48\n\t" \
  "v_sub_f32 v40, v40, v48\n\t" \
  "v_sub_f32 v41, v41, v48\n\t" \
  "v_sub_f32 v42, v42, v48\n\t" \
  "v_sub_f32 v43, v43, v48\n\t" \
  "v_sub_f32 v44, v44, v48\n\t" \
  "v_sub_f32 v45, v45, v48\n\t" \
  "v_mul_f32 v50, v36, v36\n\t" \
  "v_fmac_f32 v50, v37, v37\n\t" \
  "v_fmac_f32 v50, v38, v38\n\t" \
  "v_fmac_f32 v50, v39, v39\n\t" \
  "v_fmac_f32 v50, v40, v40\n\t" \
  "v_fmac_f32 v50, v41, v41\n\t" \
  "v_fmac_f32 v50, v42, v42\n\t" \
  "v_fmac_f32 v50, v43, v43\n\t" \
  "v_fmac_f32 v50, v44, v44\n\t" \
  "v_fmac_f32 v50, v45, v45\n\t" \
  "s_nop 1\n\t" \
  "v_add_f32_dpp v50, v50, v50 quad_perm:[1,0,3,2] row_mask:0xf bank_mask:0xf bound_ctrl:1\n\t" \
  "s_nop 1\n\t" \
  "v_add_f32_dpp v50, v50, v50 quad_perm:[2,3,0,1] row_mask:0xf bank_mask:0xf bound_ctrl:1\n\t" \
  "s_nop 1\n\t" \
  "v_add_f32_dpp v50, v50, v50 row_half_mirror row_mask:0xf bank_mask:0xf bound_ctrl:1\n\t" \
  "s_nop 1\n\t" \
  "v_add_f32_dpp v50, v50, v50 row_mirror row_mask:0xf bank_mask:0xf bound_ctrl:1\n\t" \
  "v_mov_b32 v46, v50\n\t" \
  "s_nop 1\n\t" \
  "v_permlane16_swap_b32 v50, v46\n\t" \
  "v_add_f32 v50, v50, v46\n\t" \
  "v_mov_b32 v46, v50\n\t" \
  "s_nop 1\n\t" \
  "v_permlane32_swap_b32 v50, v46\n\t" \
  "v_add_f32 v50, v50, v46\n\t" \
  "v_mul_f32 v50, 0x3acccccd, v50\n\t" \
  "s_branch .Lvardq%=\n\t" \
  ".Lvarf%=:\n\t" \
  "v_lshlrev_b32 v36, 16, v10\n\t" \
  "v_and_b32 v37, 0xffff0000, v10\n\t" \
  "v_lshlrev_b32 v38, 16, v11\n\t" \
  "v_and_b32 v39, 0xffff0000, v11\n\t" \
  "v_lshlrev_b32 v40, 16, v12\n\t" \
  "v_and_b32 v41, 0xffff0000, v12\n\t" \
  "v_lshlrev_b32 v42, 16, v13\n\t" \
  "v_and_b32 v43, 0xffff0000, v13\n\t" \
  "v_lshlrev_b32 v44, 16, v14\n\t" \
  "v_and_b32 v45, 0xffff0000, v14\n\t" \
  "v_sub_f32 v36, v36, v48\n\t" \
  "v_sub_f32 v37, v37, v48\n\t" \
  "v_sub_f32 v38, v38, v48\n\t" \
  "v_sub_f32 v39, v39, v48\n\t" \
  "v_sub_f32 v40, v40, v48\n\t" \
  "v_sub_f32 v41, v41, v48\n\t" \
  "v_sub_f32 v42, v42, v48\n\t" \
  "v_sub_f32 v43, v43, v48\n\t" \
  "v_sub_f32 v44, v44, v48\n\t" \
  "v_sub_f32 v45, v45, v48\n\t" \
  "v_mul_f32 v50, v36, v36\n\t" \
  "v_fmac_f32 v50, v37, v37\n\t" \
  "v_fmac_f32 v50, v38, v38\n\t" \
  "v_fmac_f32 v50, v39, v39\n\t" \
  "v_fmac_f32 v50, v40, v40\n\t" \
  "v_fmac_f32 v50, v41, v41\n\t" \
  "v_fmac_f32 v50, v42, v42\n\t" \
  "v_fmac_f32 v50, v43, v43\n\t" \
  "v_fmac_f32 v50, v44, v44\n\t" \
  "v_fmac_f32 v50, v45, v45\n\t" \
  "s_nop 1\n\t" \
  "v_add_f32_dpp v50, v50, v50 quad_perm:[1,0,3,2] row_mask:0xf bank_mask:0xf bound_ctrl:1\n\t" \
  "s_nop 1\n\t" \
  "v_add_f32_dpp v50, v50, v50 quad_perm:[2,3,0,1] row_mask:0xf bank_mask:0xf bound_ctrl:1\n\t" \
  "s_nop 1\n\t" \
  "v_add_f32_dpp v50, v50, v50 row_half_mirror row_mask:0xf bank_mask:0xf bound_ctrl:1\n\t" \
  "s_nop 1\n\t" \
  "v_add_f32_dpp v50, v50, v50 row_mirror row_mask:0xf bank_mask:0xf bound_ctrl:1\n\t" \
  "v_mov_b32 v46, v50\n\t" \
  "s_nop 1\n\t" \
  "v_permlane16_swap_b32 v50, v46\n\t" \
  "v_add_f32 v50, v50, v46\n\t" \
  "v_mov_b32 v46, v50\n\t" \
  "s_nop 1\n\t" \
  "v_permlane32_swap_b32 v50, v46\n\t" \
  "v_add_f32 v50, v50, v46\n\t" \
  "v_mul_f32 v50, 0x3acccccd, v50\n\t" \
  "s_branch .Lvardf%=\n\t" \
  ".Lstqa%=:\n\t" \
  "s_mov_b32 s54, 0x400000\n\t" \
  ".Lstqc%=:\n\t" \
  "s_sleep 2\n\t" \
  "global_load_dwordx4 v[240:243], v180, s[46:47] sc1\n\t" \
  "global_load_dwordx4 v[244:247], v181, s[46:47] sc1\n\t" \
  "global_load_dwordx4 v[248:251], v182, s[46:47] sc1\n\t" \
  "global_load_dwordx4 v[252:255], v183, s[46:47] sc1\n\t" \
  "s_waitcnt vmcnt(0)\n\t" \
  "v_xor_b32 v188, s28, v241\n\t" \
  "v_xor_b32 v189, s28, v243\n\t" \
  "v_bitop3_b32 v188, v188, s28, v245 bitop3:0xf6\n\t" \
  "v_bitop3_b32 v189, v189, s28, v247 bitop3:0xf6\n\t" \
  "v_bitop3_b32 v188, v188, s28, v249 bitop3:0xf6\n\t" \
  "v_bitop3_b32 v189, v189, s28, v251 bitop3:0xf6\n\t" \
  "v_bitop3_b32 v188, v188, s28, v253 bitop3:0xf6\n\t" \
  "v_bitop3_b32 v189, v189, s28, v255 bitop3:0xf6\n\t" \
  "v_or_b32 v188, v188, v189\n\t" \
  "v_cmp_ne_u32 vcc, 0, v188\n\t" \
  "s_nop 1\n\t" \
  "s_cbranch_vccz .Lstqb%=\n\t" \
  "s_sub_u32 s54, s54, 1\n\t" \
  "s_cbranch_scc0 .Lstqc%=\n\t" \
  "s_branch .Lstqb%=\n\t" \
  ".Lq2%=:\n\t" \
  "v_mov_b64 v[188:189], -1\n\t" \
  "v_mov_b64 v[190:191], -1\n\t" \
  "v_and_b32 v24, 15, v228\n\t" \
  "v_lshrrev_b32 v25, 4, v228\n\t" \
  "v_and_b32 v229, 48, v228\n\t" \
  "v_bfe_u32 v26, v228, 2, 2\n\t" \
  "v_and_b32 v28, 3, v228\n\t" \
  "v_lshlrev_b32 v27, 6, v26\n\t" \
  "v_lshl_or_b32 v27, v28, 3, v27\n\t" \
  "v_lshlrev_b32 v29, 1, v229\n\t" \
  "v_and_b32 v29, 32, v29\n\t" \
  "v_or_b32 v27, v27, v29\n\t" \
  "v_lshrrev_b32 v30, 1, v24\n\t" \
  "v_xor_b32 v30, v30, v25\n\t" \
  "v_lshlrev_b32 v30, 4, v30\n\t" \
  "v_lshl_or_b32 v30, v24, 7, v30\n\t" \
  "v_cmp_gt_u32 vcc, 2, v24\n\t" \
  "v_mov_b32 v33, 0x3f803f80\n\t" \
  "v_mov_b32 v174, 0xeeeeeeee\n\t" \
  "v_cndmask_b32 v176, 0, v33, vcc\n\t" \
  "v_mov_b32 v34, 0x44444444\n\t" \
  "v_cmp_eq_u32 vcc, 0, v24\n\t" \
  "v_mov_b32 v177, v176\n\t" \
  "v_mov_b64 v[178:179], v[176:177]\n\t" \
  "v_cndmask_b32 v174, v174, v34, vcc\n\t" \
  "v_lshl_add_u32 v164, s68, 13, v30\n\t" \
  "v_xor_b32 v166, 64, v164\n\t" \
  "v_mul_u32_u24 v31, 0x120, v28\n\t" \
  "v_and_b32 v31, 0x220, v31\n\t" \
  "v_and_b32 v32, 0xc0, v237\n\t" \
  "v_or_b32 v180, v31, v32\n\t" \
  "v_mul_u32_u24 v25, 3072, v25\n\t" \
  "v_add_u32 v25, 16384, v25\n\t" \
  "s_mul_i32 s63, s34, 3\n\t" \
  "s_cmp_eq_u32 s68, 0\n\t" \
  "s_cselect_b32 s64, 0x24, 0x12\n\t" \
  "s_lshl_b32 s65, 1, s34\n\t" \
  "s_andn2_b32 s65, s65, 1\n\t" \
  "s_cmp_ge_u32 s33, 6\n\t" \
  "s_cmov_b32 s63, s65\n\t" \
  "s_cmov_b32 s64, 0x14\n\t" \
  "s_bfe_u32 s65, s64, 131072\n\t" \
  "s_add_u32 s3, s63, s65\n\t" \
  "s_mul_i32 s65, s3, 0x108\n\t" \
  "s_and_b32 s65, s65, 0xe00\n\t" \
  "s_bitcmp1_b32 s3, 0\n\t" \
  "s_cselect_b32 s66, 32, 0\n\t" \
  "v_xor_b32 v160, s66, v27\n\t" \
  "v_add3_u32 v160, v160, s65, v25\n\t" \
  "s_bfe_u32 s65, s64, 131074\n\t" \
  "s_add_u32 s4, s63, s65\n\t" \
  "s_mul_i32 s65, s4, 0x108\n\t" \
  "s_and_b32 s65, s65, 0xe00\n\t" \
  "s_bitcmp1_b32 s4, 0\n\t" \
  "s_cselect_b32 s66, 32, 0\n\t" \
  "v_xor_b32 v161, s66, v27\n\t" \
  "v_add3_u32 v161, v161, s65, v25\n\t" \
  "s_bfe_u32 s65, s64, 131076\n\t" \
  "s_add_u32 s5, s63, s65\n\t" \
  "s_mul_i32 s65, s5, 0x108\n\t" \
  "s_and_b32 s65, s65, 0xe00\n\t" \
  "s_bitcmp1_b32 s5, 0\n\t" \
  "s_cselect_b32 s66, 32, 0\n\t" \
  "v_xor_b32 v162, s66, v27\n\t" \
  "v_add3_u32 v162, v162, s65, v25\n\t" \
  "s_and_b32 s64, s33, 1\n\t" \
  "s_lshl1_add_u32 s64, s64, s68\n\t" \
  "s_lshl2_add_u32 s64, s57, s64\n\t" \
  "v_xor_b32 v181, 16, v180\n\t" \
  "v_xor_b32 v182, 256, v180\n\t" \
  "v_xor_b32 v183, 272, v180\n\t" \
  "s_mov_b32 s71, s64\n\t" \
  "s_lshl_b32 s64, s64, 10\n\t" \
  "s_add_u32 s64, s64, 4096\n\t" \
  "s_add_u32 s46, s24, s64\n\t" \
  "s_addc_u32 s47, s25, 0\n\t" \
  "s_lshl_b32 s63, s60, 15\n\t" \
  "s_add_u32 s63, s63, 65536\n\t" \
  "s_add_u32 s30, s24, s63\n\t" \
  "s_addc_u32 s31, s25, 0\n\t" \
  "s_lshl_b32 s62, s60, 5\n\t" \
  "s_lshr_b32 s63, s61, 4\n\t" \
  "s_or_b32 s63, s63, s62\n\t" \
  "v_and_or_b32 v233, v228, 6, s63\n\t" \
  "s_lshl_b32 s63, s32, 6\n\t" \
  "s_add_u32 s63, s63, 6\n\t" \
  "v_and_or_b32 v31, v237, 32, s63\n\t" \
  "s_mov_b32 exec_lo, 0\n\t" \
  "v_and_or_b32 v233, v228, 1, v31\n\t" \
  "s_mov_b32 exec_lo, -1\n\t" \
  "v_lshlrev_b32 v233, 7, v233\n\t" \
  "s_add_u32 s62, s62, s61\n\t" \
  "s_lshl_b32 s63, s57, 5\n\t" \
  "s_add_u32 s63, s63, s33\n\t" \
  "s_lshl_b32 s63, s63, 7\n\t" \
  "s_add_u32 s63, s63, 65536\n\t" \
  "v_lshrrev_b32 v31, 3, v228\n\t" \
  "v_lshl_add_u32 v31, v31, 15, s63\n\t" \
  "v_and_b32 v32, 0x70, v237\n\t" \
  "v_add_u32 v231, v31, v32\n\t" \
  "v_lshl_add_u32 v186, s32, 16, v231\n\t" \
  "s_lshl3_add_u32 s63, s35, s32\n\t" \
  "s_mul_i32 s64, s63, 34\n\t" \
  "v_min_u32 v31, 33, v228\n\t" \
  "v_add_u32 v31, s64, v31\n\t" \
  "v_min_u32 v31, 17279, v31\n\t" \
  "v_lshlrev_b32 v187, 7, v31\n\t" \
  "s_mul_i32 s64, s63, 13\n\t" \
  "v_min_u32 v31, 12, v228\n\t" \
  "v_add_u32 v31, s64, v31\n\t" \
  "v_min_u32 v31, 6399, v31\n\t" \
  "v_lshlrev_b32 v193, 7, v31\n\t" \
  "s_add_u32 m0, s48, s56\n\t" \
  "s_nop 0\n\t" \
  "global_load_lds_dwordx4 v170, s[36:37]\n\t" \
  "s_add_u32 m0, m0, 0x400\n\t" \
  "v_add_u32 v170, 0x80, v170\n\t" \
  "global_load_lds_dwordx4 v171, s[36:37]\n\t" \
  "v_add_u32 v171, 0x80, v171\n\t" \
  "s_barrier\n\t" \
  "s_mov_b32 s56, 53248\n\t" \
  "s_add_u32 m0, s50, s56\n\t" \
  "s_nop 0\n\t" \
  "global_load_lds_dwordx4 v172, s[40:41]\n\t" \
  "s_add_u32 m0, m0, 0x400\n\t" \
  "v_add_u32 v172, 81920, v172\n\t" \
  "global_load_lds_dwordx4 v173, s[42:43]\n\t" \
  "s_add_u32 m0, m0, 0x400\n\t" \
  "s_nop 0\n\t" \
  "global_load_lds_dwordx4 v173, s[44:45]\n\t" \
  "v_add_u32 v173, s52, v173\n\t" \
  "s_waitcnt vmcnt(8)\n\t" \
  "v_dot2_f32_bf16 v35, v10, v33, 0\n\t" \
  "v_dot2_f32_bf16 v36, v10, v10, 0\n\t" \
  "v_dot2_f32_bf16 v35, v11, v33, v35\n\t" \
  "v_dot2_f32_bf16 v36, v11, v11, v36\n\t" \
  "v_dot2_f32_bf16 v35, v12, v33, v35\n\t" \
  "v_dot2_f32_bf16 v36, v12, v12, v36\n\t" \
  "v_dot2_f32_bf16 v35, v13, v33, v35\n\t" \
  "v_dot2_f32_bf16 v36, v13, v13, v36\n\t" \
  "v_dot2_f32_bf16 v35, v14, v33, v35\n\t" \
  "v_dot2_f32_bf16 v36, v14, v14, v36\n\t" \
  "v_readfirstlane_b32 s66, v16\n\t" \
  "v_readfirstlane_b32 s67, v17\n\t" \
  "s_lshl_b32 s63, s69, 4\n\t" \
  "v_add_f32_dpp v35, v35, v35 quad_perm:[1,0,3,2] row_mask:0xf bank_mask:0xf bound_ctrl:1\n\t" \
  "v_add_f32_dpp v36, v36, v36 quad_perm:[1,0,3,2] row_mask:0xf bank_mask:0xf bound_ctrl:1\n\t" \
  "s_add_u32 s63, s63, 4096\n\t" \
  "v_add_f32_dpp v35, v35, v35 quad_perm:[2,3,0,1] row_mask:0xf bank_mask:0xf bound_ctrl:1\n\t" \
  "v_add_f32_dpp v36, v36, v36 quad_perm:[2,3,0,1] row_mask:0xf bank_mask:0xf bound_ctrl:1\n\t" \
  "v_mov_b32 v9, s63\n\t" \
  "v_add_f32_dpp v35, v35, v35 row_half_mirror row_mask:0xf bank_mask:0xf bound_ctrl:1\n\t" \
  "v_add_f32_dpp v36, v36, v36 row_half_mirror row_mask:0xf bank_mask:0xf bound_ctrl:1\n\t" \
  "s_mul_i32 s63, s67, 0x4c957f2d\n\t" \
  "v_add_f32_dpp v35, v35, v35 row_mirror row_mask:0xf bank_mask:0xf bound_ctrl:1\n\t" \
  "v_add_f32_dpp v36, v36, v36 row_mirror row_mask:0xf bank_mask:0xf bound_ctrl:1\n\t" \
  "s_mul_i32 s64, s66, 0x5851f42d\n\t" \
  "v_mov_b32 v46, v35\n\t" \
  "v_mov_b32 v47, v36\n\t" \
  "s_add_u32 s63, s63, s64\n\t" \
  "v_permlane16_swap_b32 v35, v46\n\t" \
  "v_permlane16_swap_b32 v36, v47\n\t" \
  "s_mul_hi_u32 s64, s66, 0x4c957f2d\n\t" \
  "v_add_f32 v35, v35, v46\n\t" \
  "v_add_f32 v36, v36, v47\n\t" \
  "s_add_u32 s63, s63, s64\n\t" \
  "v_mov_b32 v46, v35\n\t" \
  "v_mov_b32 v47, v36\n\t" \
  "s_mul_i32 s64, s66, 0x4c957f2d\n\t" \
  "v_permlane32_swap_b32 v35, v46\n\t" \
  "v_permlane32_swap_b32 v36, v47\n\t" \
  "s_add_u32 s28, s64, 0xf767814f\n\t" \
  "v_add_f32 v35, v35, v46\n\t" \
  "v_add_f32 v36, v36, v47\n\t" \
  "s_addc_u32 s29, s63, 0x14057b7e\n\t" \
  "v_mul_f32 v48, 0x3acccccd, v35\n\t" \
  "v_mul_f32 v36, 0x3acccccd, v36\n\t" \
  "v_fma_f32 v50, -v48, v48, v36\n\t" \
  "v_mul_f32 v36, 0x3b800000, v36\n\t" \
  "v_cmp_gt_f32 vcc, v36, v50\n\t" \
  "v_mov_b32 v49, s28\n\t" \
  "v_mov_b32 v51, s28\n\t" \
  "s_cbranch_vccnz .Lvarq%=\n\t" \
  ".Lvardq%=:\n\t" \
  "v_add_f32 v50, 0x3727c5ac, v50\n\t" \
  "v_mov_b64 v[80:81], 0\n\t" \
  "v_rsq_f32 v50, v50\n\t" \
  "s_mov_b32 s53, 0\n\t" \
  "s_mov_b32 s54, 0\n\t" \
  "s_mov_b32 s55, 53248\n\t" \
  "s_mov_b32 s66, -106496\n\t" \
  "s_mov_b32 s67, 53248\n\t" \
  "s_add_u32 m0, s48, s56\n\t" \
  "s_nop 0\n\t" \
  "global_load_lds_dwordx4 v170, s[36:37]\n\t" \
  "s_add_u32 m0, m0, 0x400\n\t" \
  "v_add_u32 v170, 0x80, v170\n\t" \
  "global_load_lds_dwordx4 v171, s[36:37]\n\t" \
  "v_add_u32 v171, 0x80, v171\n\t" \
  "s_mov_b64 exec, 0xffff\n\t" \
  "global_store_dwordx4 v186, v[188:191], s[24:25] sc1\n\t" \
  "s_mov_b64 exec, -1\n\t" \
  "s_mov_b32 s56, 106496\n\t" \
  "s_mov_b64 exec, 1\n\t" \
  "global_store_dwordx4 v9, v[48:51], s[24:25] sc1\n\t" \
  "s_mov_b64 exec, -1\n\t" \
  "v_mfma_f32_32x32x8_bf16 v[64:79], v[80:81], v[80:81], 0\n\t" \
  "v_mfma_f32_32x32x8_bf16 v[32:47], v[80:81], v[80:81], 0\n\t" \
  "v_mfma_f32_32x32x8_bf16 v[16:31], v[80:81], v[80:81], 0\n\t" \
  "v_mfma_f32_32x32x8_bf16 v[0:15], v[80:81], v[80:81], 0\n\t" \
  "s_waitcnt vmcnt(7)\n\t" \
  "s_barrier\n\t" \
  "ds_read_b128 v[112:115], v164 offset:0\n\t" \
  "ds_read_b64_tr_b16 v[80:81], v160 offset:0\n\t" \
  "ds_read_b64_tr_b16 v[82:83], v160 offset:256\n\t" \
  "ds_read_b128 v[116:119], v164 offset:2048\n\t" \
  "ds_read_b128 v[120:123], v164 offset:4096\n\t" \
  "ds_read_b128 v[124:127], v164 offset:6144\n\t" \
  "ds_read_b64_tr_b16 v[88:89], v161 offset:0\n\t" \
  "ds_read_b64_tr_b16 v[90:91], v161 offset:256\n\t" \
  "ds_read_b64_tr_b16 v[96:97], v162 offset:0\n\t" \
  "ds_read_b64_tr_b16 v[98:99], v162 offset:256\n\t" \
  "s_waitcnt lgkmcnt(4)\n\t" \
  "v_mfma_f32_16x16x32_bf16 v[0:3], v[80:83], v[112:115], v[0:3]\n\t" \
  "ds_read_b128 v[128:131], v166 offset:0\n\t" \
  "s_add_u32 m0, s50, s56\n\t" \
  "v_mfma_f32_16x16x32_bf16 v[4:7], v[80:83], v[116:119], v[4:7]\n\t" \
  "ds_read_b64_tr_b16 v[84:85], v160 offset:12288\n\t" \
  "global_load_lds_dwordx4 v172, s[40:41]\n\t" \
  "v_mfma_f32_16x16x32_bf16 v[8:11], v[80:83], v[120:123], v[8:11]\n\t" \
  "ds_read_b64_tr_b16 v[86:87], v160 offset:12544\n\t" \
  "s_add_u32 m0, m0, 0x400\n\t" \
  "v_mfma_f32_16x16x32_bf16 v[12:15], v[80:83], v[124:127], v[12:15]\n\t" \
  "ds_read_b128 v[132:135], v166 offset:2048\n\t" \
  "s_waitcnt lgkmcnt(4)\n\t" \
  "v_mfma_f32_16x16x32_bf16 v[16:19], v[88:91], v[112:115], v[16:19]\n\t" \
  "ds_read_b128 v[136:139], v166 offset:4096\n\t" \
  "v_add_u32 v172, 81920, v172\n\t" \
  "v_mfma_f32_16x16x32_bf16 v[20:23], v[88:91], v[116:119], v[20:23]\n\t" \
  "ds_read_b128 v[140:143], v166 offset:6144\n\t" \
  "global_load_lds_dwordx4 v173, s[42:43]\n\t" \
  "v_mfma_f32_16x16x32_bf16 v[24:27], v[88:91], v[120:123], v[24:27]\n\t" \
  "ds_read_b64_tr_b16 v[92:93], v161 offset:12288\n\t" \
  "s_add_u32 m0, m0, 0x400\n\t" \
  "v_mfma_f32_16x16x32_bf16 v[28:31], v[88:91], v[124:127], v[28:31]\n\t" \
  "ds_read_b64_tr_b16 v[94:95], v161 offset:12544\n\t" \
  "global_load_lds_dwordx4 v173, s[44:45]\n\t" \
  "v_mfma_f32_16x16x32_bf16 v[32:35], v[96:99], v[112:115], v[32:35]\n\t" \
  "ds_read_b64_tr_b16 v[100:101], v162 offset:12288\n\t" \
  "v_add_u32 v173, s52, v173\n\t" \
  "v_mfma_f32_16x16x32_bf16 v[36:39], v[96:99], v[116:119], v[36:39]\n\t" \
  "ds_read_b64_tr_b16 v[102:103], v162 offset:12544\n\t" \
  "s_add_u32 m0, s48, s56\n\t" \
  "v_mfma_f32_16x16x32_bf16 v[40:43], v[96:99], v[120:123], v[40:43]\n\t" \
  "global_load_lds_dwordx4 v170, s[36:37]\n\t" \
  "s_add_u32 m0, m0, 0x400\n\t" \
  "v_mfma_f32_16x16x32_bf16 v[44:47], v[96:99], v[124:127], v[44:47]\n\t" \
  "v_add_u32 v170, 0x80, v170\n\t" \
  "s_waitcnt lgkmcnt(4)\n\t" \
  "v_smfmac_f32_16x16x64_bf16 v[72:75], v[176:179], v[80:87], v174\n\t" \
  "global_load_lds_dwordx4 v171, s[36:37]\n\t" \
  "v_mfma_f32_16x16x32_bf16 v[0:3], v[84:87], v[128:131], v[0:3]\n\t" \
  "v_add_u32 v171, 0x80, v171\n\t" \
  "v_add_u32 v164, s55, v164\n\t" \
  "v_mfma_f32_16x16x32_bf16 v[4:7], v[84:87], v[132:135], v[4:7]\n\t" \
  "s_mov_b32 s56, 0\n\t" \
  "v_add_u32 v160, s55, v160\n\t" \
  "v_mfma_f32_16x16x32_bf16 v[8:11], v[84:87], v[136:139], v[8:11]\n\t" \
  "v_add_u32 v161, s55, v161\n\t" \
  "v_add_u32 v162, s55, v162\n\t" \
  "s_setprio 1\n\t" \
  "s_waitcnt vmcnt(7) lgkmcnt(0)\n\t" \
  "s_branch .Lg1yq%=\n\t" \
  ".Lg1aq%=:\n\t" \
  "v_mfma_f32_16x16x32_bf16 v[0:3], v[80:83], v[112:115], v[0:3]\n\t" \
  "ds_read_b128 v[128:131], v166 offset:0\n\t" \
  "s_setprio 0\n\t" \
  "v_mfma_f32_16x16x32_bf16 v[4:7], v[80:83], v[116:119], v[4:7]\n\t" \
  "ds_read_b64_tr_b16 v[84:85], v160 offset:12288\n\t" \
  "s_add_u32 m0, s48, s56\n\t" \
  "v_mfma_f32_16x16x32_bf16 v[8:11], v[80:83], v[120:123], v[8:11]\n\t" \
  "ds_read_b64_tr_b16 v[86:87], v160 offset:12544\n\t" \
  "global_load_lds_dwordx4 v170, s[36:37]\n\t" \
  "v_mfma_f32_16x16x32_bf16 v[12:15], v[80:83], v[124:127], v[12:15]\n\t" \
  "ds_read_b128 v[132:135], v166 offset:2048\n\t" \
  "s_waitcnt lgkmcnt(4)\n\t" \
  "v_mfma_f32_16x16x32_bf16 v[16:19], v[88:91], v[112:115], v[16:19]\n\t" \
  "ds_read_b128 v[136:139], v166 offset:4096\n\t" \
  "s_add_u32 m0, m0, 0x400\n\t" \
  "v_mfma_f32_16x16x32_bf16 v[20:23], v[88:91], v[116:119], v[20:23]\n\t" \
  "ds_read_b128 v[140:143], v166 offset:6144\n\t" \
  "v_add_u32 v170, 0x80, v170\n\t" \
  "v_mfma_f32_16x16x32_bf16 v[24:27], v[88:91], v[120:123], v[24:27]\n\t" \
  "ds_read_b64_tr_b16 v[92:93], v161 offset:12288\n\t" \
  "global_load_lds_dwordx4 v171, s[36:37]\n\t" \
  "v_mfma_f32_16x16x32_bf16 v[28:31], v[88:91], v[124:127], v[28:31]\n\t" \
  "ds_read_b64_tr_b16 v[94:95], v161 offset:12544\n\t" \
  "v_add_u32 v171, 0x80, v171\n\t" \
  "v_mfma_f32_16x16x32_bf16 v[32:35], v[96:99], v[112:115], v[32:35]\n\t" \
  "ds_read_b64_tr_b16 v[100:101], v162 offset:12288\n\t" \
  "v_add_u32 v164, s55, v164\n\t" \
  "v_mfma_f32_16x16x32_bf16 v[36:39], v[96:99], v[116:119], v[36:39]\n\t" \
  "ds_read_b64_tr_b16 v[102:103], v162 offset:12544\n\t" \
  "s_mov_b32 s56, s53\n\t" \
  "v_mfma_f32_16x16x32_bf16 v[40:43], v[96:99], v[120:123], v[40:43]\n\t" \
  "s_cmp_ge_u32 s54, 7\n\t" \
  "v_add_u32 v160, s55, v160\n\t" \
  "v_mfma_f32_16x16x32_bf16 v[44:47], v[96:99], v[124:127], v[44:47]\n\t" \
  "v_add_u32 v161, s55, v161\n\t" \
  "s_waitcnt lgkmcnt(4)\n\t" \
  "v_smfmac_f32_16x16x64_bf16 v[72:75], v[176:179], v[80:87], v174\n\t" \
  "v_add_u32 v162, s55, v162\n\t" \
  "v_mfma_f32_16x16x32_bf16 v[0:3], v[84:87], v[128:131], v[0:3]\n\t" \
  "v_mfma_f32_16x16x32_bf16 v[4:7], v[84:87], v[132:135], v[4:7]\n\t" \
  "v_mfma_f32_16x16x32_bf16 v[8:11], v[84:87], v[136:139], v[8:11]\n\t" \
  "s_setprio 1\n\t" \
  "s_waitcnt vmcnt(5) lgkmcnt(0)\n\t" \
  "s_cbranch_scc1 .Lg1tq%=\n\t" \
  ".Lg1yq%=:\n\t" \
  "s_barrier\n\t" \
  "v_smfmac_f32_16x16x64_bf16 v[76:79], v[176:179], v[88:95], v174\n\t" \
  "ds_read_b128 v[112:115], v164 offset:0\n\t" \
  "v_mfma_f32_16x16x32_bf16 v[12:15], v[84:87], v[140:143], v[12:15]\n\t" \
  "ds_read_b64_tr_b16 v[80:81], v160 offset:0\n\t" \
  "s_add_u32 m0, s50, s56\n\t" \
  "v_mfma_f32_16x16x32_bf16 v[16:19], v[92:95], v[128:131], v[16:19]\n\t" \
  "ds_read_b64_tr_b16 v[82:83], v160 offset:256\n\t" \
  "global_load_lds_dwordx4 v172, s[40:41]\n\t" \
  "v_mfma_f32_16x16x32_bf16 v[20:23], v[92:95], v[132:135], v[20:23]\n\t" \
  "ds_read_b128 v[116:119], v164 offset:2048\n\t" \
  "s_add_u32 m0, m0, 0x400\n\t" \
  "v_mfma_f32_16x16x32_bf16 v[24:27], v[92:95], v[136:139], v[24:27]\n\t" \
  "ds_read_b128 v[120:123], v164 offset:4096\n\t" \
  "v_add_u32 v172, 81920, v172\n\t" \
  "v_mfma_f32_16x16x32_bf16 v[28:31], v[92:95], v[140:143], v[28:31]\n\t" \
  "ds_read_b128 v[124:127], v164 offset:6144\n\t" \
  "global_load_lds_dwordx4 v173, s[42:43]\n\t" \
  "v_mfma_f32_16x16x32_bf16 v[32:35], v[100:103], v[128:131], v[32:35]\n\t" \
  "ds_read_b64_tr_b16 v[88:89], v161 offset:0\n\t" \
  "s_add_u32 m0, m0, 0x400\n\t" \
  "v_mfma_f32_16x16x32_bf16 v[36:39], v[100:103], v[132:135], v[36:39]\n\t" \
  "ds_read_b64_tr_b16 v[90:91], v161 offset:256\n\t" \
  "global_load_lds_dwordx4 v173, s[44:45]\n\t" \
  "v_mfma_f32_16x16x32_bf16 v[40:43], v[100:103], v[136:139], v[40:43]\n\t" \
  "ds_read_b64_tr_b16 v[96:97], v162 offset:0\n\t" \
  "v_add_u32 v173, s52, v173\n\t" \
  "v_mfma_f32_16x16x32_bf16 v[44:47], v[100:103], v[140:143], v[44:47]\n\t" \
  "ds_read_b64_tr_b16 v[98:99], v162 offset:256\n\t" \
  "v_add_u32 v166, s55, v166\n\t" \
  "s_add_u32 s53, s53, s55\n\t" \
  "s_add_u32 s54, s54, 1\n\t" \
  "s_cmp_eq_u32 s53, 106496\n\t" \
  "s_cselect_b32 s55, s66, s67\n\t" \
  "s_waitcnt lgkmcnt(4)\n\t" \
  "s_branch .Lg1aq%=\n\t" \
  ".Lg1tq%=:\n\t" \
  "s_barrier\n\t" \
  "v_smfmac_f32_16x16x64_bf16 v[76:79], v[176:179], v[88:95], v174\n\t" \
  "ds_read_b128 v[112:115], v164 offset:0\n\t" \
  "v_mfma_f32_16x16x32_bf16 v[12:15], v[84:87], v[140:143], v[12:15]\n\t" \
  "ds_read_b64_tr_b16 v[80:81], v160 offset:0\n\t" \
  "global_load_dwordx4 v[240:243], v180, s[46:47] sc1\n\t" \
  "v_mfma_f32_16x16x32_bf16 v[16:19], v[92:95], v[128:131], v[16:19]\n\t" \
  "ds_read_b64_tr_b16 v[82:83], v160 offset:256\n\t" \
  "global_load_dwordx4 v[244:247], v181, s[46:47] sc1\n\t" \
  "v_mfma_f32_16x16x32_bf16 v[20:23], v[92:95], v[132:135], v[20:23]\n\t" \
  "ds_read_b128 v[116:119], v164 offset:2048\n\t" \
  "global_load_dwordx4 v[248:251], v182, s[46:47] sc1\n\t" \
  "v_mfma_f32_16x16x32_bf16 v[24:27], v[92:95], v[136:139], v[24:27]\n\t" \
  "ds_read_b128 v[120:123], v164 offset:4096\n\t" \
  "global_load_dwordx4 v[252:255], v183, s[46:47] sc1\n\t" \
  "v_mfma_f32_16x16x32_bf16 v[28:31], v[92:95], v[140:143], v[28:31]\n\t" \
  "ds_read_b128 v[124:127], v164 offset:6144\n\t" \
  "v_add_u32 v166, s55, v166\n\t" \
  "v_mfma_f32_16x16x32_bf16 v[32:35], v[100:103], v[128:131], v[32:35]\n\t" \
  "ds_read_b64_tr_b16 v[88:89], v161 offset:0\n\t" \
  "s_mov_b32 s55, s66\n\t" \
  "v_mfma_f32_16x16x32_bf16 v[36:39], v[100:103], v[132:135], v[36:39]\n\t" \
  "ds_read_b64_tr_b16 v[90:91], v161 offset:256\n\t" \
  "v_mfma_f32_16x16x32_bf16 v[40:43], v[100:103], v[136:139], v[40:43]\n\t" \
  "ds_read_b64_tr_b16 v[96:97], v162 offset:0\n\t" \
  "v_mfma_f32_16x16x32_bf16 v[44:47], v[100:103], v[140:143], v[44:47]\n\t" \
  "ds_read_b64_tr_b16 v[98:99], v162 offset:256\n\t" \
  "s_waitcnt lgkmcnt(4)\n\t" \
  "v_mfma_f32_16x16x32_bf16 v[0:3], v[80:83], v[112:115], v[0:3]\n\t" \
  "ds_read_b128 v[128:131], v166 offset:0\n\t" \
  "s_setprio 0\n\t" \
  "v_mfma_f32_16x16x32_bf16 v[4:7], v[80:83], v[116:119], v[4:7]\n\t" \
  "ds_read_b64_tr_b16 v[84:85], v160 offset:12288\n\t" \
  "v_add_u32 v164, s55, v164\n\t" \
  "v_mfma_f32_16x16x32_bf16 v[8:11], v[80:83], v[120:123], v[8:11]\n\t" \
  "ds_read_b64_tr_b16 v[86:87], v160 offset:12544\n\t" \
  "s_lshr_b32 s63, s33, 1\n\t" \
  "v_mfma_f32_16x16x32_bf16 v[12:15], v[80:83], v[124:127], v[12:15]\n\t" \
  "ds_read_b128 v[132:135], v166 offset:2048\n\t" \
  "s_waitcnt lgkmcnt(4)\n\t" \
  "v_mfma_f32_16x16x32_bf16 v[16:19], v[88:91], v[112:115], v[16:19]\n\t" \
  "ds_read_b128 v[136:139], v166 offset:4096\n\t" \
  "s_lshl2_add_u32 s42, s63, s34\n\t" \
  "v_mfma_f32_16x16x32_bf16 v[20:23], v[88:91], v[116:119], v[20:23]\n\t" \
  "ds_read_b128 v[140:143], v166 offset:6144\n\t" \
  "s_mov_b32 s41, 0x3ea52b5b\n\t" \
  "v_mfma_f32_16x16x32_bf16 v[24:27], v[88:91], v[120:123], v[24:27]\n\t" \
  "ds_read_b64_tr_b16 v[92:93], v161 offset:12288\n\t" \
  "s_cmp_ge_u32 s42, 14\n\t" \
  "s_cmov_b32 s41, 1.0\n\t" \
  "v_mfma_f32_16x16x32_bf16 v[28:31], v[88:91], v[124:127], v[28:31]\n\t" \
  "ds_read_b64_tr_b16 v[94:95], v161 offset:12544\n\t" \
  "v_bfe_u32 v194, v228, 2, 2\n\t" \
  "v_mfma_f32_16x16x32_bf16 v[32:35], v[96:99], v[112:115], v[32:35]\n\t" \
  "ds_read_b64_tr_b16 v[100:101], v162 offset:12288\n\t" \
  "v_mul_u32_u24 v194, 10240, v194\n\t" \
  "v_mfma_f32_16x16x32_bf16 v[36:39], v[96:99], v[116:119], v[36:39]\n\t" \
  "ds_read_b64_tr_b16 v[102:103], v162 offset:12544\n\t" \
  "v_and_or_b32 v194, v237, 48, v194\n\t" \
  "v_mfma_f32_16x16x32_bf16 v[40:43], v[96:99], v[120:123], v[40:43]\n\t" \
  "v_lshl_add_u32 v194, v229, 2, v194\n\t" \
  "v_lshl_add_u32 v175, s3, 6, v229\n\t" \
  "v_mfma_f32_16x16x32_bf16 v[44:47], v[96:99], v[124:127], v[44:47]\n\t" \
  "v_lshl_add_u32 v196, s3, 8, v194\n\t" \
  "s_waitcnt lgkmcnt(4)\n\t" \
  "v_smfmac_f32_16x16x64_bf16 v[72:75], v[176:179], v[80:87], v174\n\t" \
  "v_add_u32 v160, s55, v160\n\t" \
  "v_mfma_f32_16x16x32_bf16 v[0:3], v[84:87], v[128:131], v[0:3]\n\t" \
  "v_add_u32 v161, s55, v161\n\t" \
  "v_add_u32 v162, s55, v162\n\t" \
  "v_mfma_f32_16x16x32_bf16 v[4:7], v[84:87], v[132:135], v[4:7]\n\t" \
  "v_mfma_f32_16x16x32_bf16 v[8:11], v[84:87], v[136:139], v[8:11]\n\t" \
  "s_setprio 1\n\t" \
  "s_waitcnt vmcnt(4) lgkmcnt(0)\n\t" \
  "s_barrier\n\t" \
  "v_smfmac_f32_16x16x64_bf16 v[76:79], v[176:179], v[88:95], v174\n\t" \
  "ds_read_b128 v[112:115], v164 offset:0\n\t" \
  "v_mfma_f32_16x16x32_bf16 v[12:15], v[84:87], v[140:143], v[12:15]\n\t" \
  "ds_read_b64_tr_b16 v[80:81], v160 offset:0\n\t" \
  "global_load_dword v187, v187, s[22:23]\n\t" \
  "v_mfma_f32_16x16x32_bf16 v[16:19], v[92:95], v[128:131], v[16:19]\n\t" \
  "ds_read_b64_tr_b16 v[82:83], v160 offset:256\n\t" \
  "global_load_dword v193, v193, s[16:17]\n\t" \
  "v_mfma_f32_16x16x32_bf16 v[20:23], v[92:95], v[132:135], v[20:23]\n\t" \
  "ds_read_b128 v[116:119], v164 offset:2048\n\t" \
  "v_add_u32 v166, s55, v166\n\t" \
  "v_mfma_f32_16x16x32_bf16 v[24:27], v[92:95], v[136:139], v[24:27]\n\t" \
  "ds_read_b128 v[120:123], v164 offset:4096\n\t" \
  "v_lshl_add_u32 v232, s4, 6, v229\n\t" \
  "v_mfma_f32_16x16x32_bf16 v[28:31], v[92:95], v[140:143], v[28:31]\n\t" \
  "ds_read_b128 v[124:127], v164 offset:6144\n\t" \
  "v_lshl_add_u32 v197, s4, 8, v194\n\t" \
  "v_mfma_f32_16x16x32_bf16 v[32:35], v[100:103], v[128:131], v[32:35]\n\t" \
  "ds_read_b64_tr_b16 v[88:89], v161 offset:0\n\t" \
  "v_lshl_add_u32 v172, s5, 6, v229\n\t" \
  "v_mfma_f32_16x16x32_bf16 v[36:39], v[100:103], v[132:135], v[36:39]\n\t" \
  "ds_read_b64_tr_b16 v[90:91], v161 offset:256\n\t" \
  "v_lshl_add_u32 v198, s5, 8, v194\n\t" \
  "v_mfma_f32_16x16x32_bf16 v[40:43], v[100:103], v[136:139], v[40:43]\n\t" \
  "ds_read_b64_tr_b16 v[96:97], v162 offset:0\n\t" \
  "v_lshl_add_u32 v173, s3, 6, v192\n\t" \
  "v_mfma_f32_16x16x32_bf16 v[44:47], v[100:103], v[140:143], v[44:47]\n\t" \
  "ds_read_b64_tr_b16 v[98:99], v162 offset:256\n\t" \
  "v_lshl_add_u32 v184, s4, 6, v192\n\t" \
  "s_waitcnt lgkmcnt(4)\n\t" \
  "v_mfma_f32_16x16x32_bf16 v[0:3], v[80:83], v[112:115], v[0:3]\n\t" \
  "ds_read_b128 v[128:131], v166 offset:0\n\t" \
  "s_setprio 0\n\t" \
  "v_mfma_f32_16x16x32_bf16 v[4:7], v[80:83], v[116:119], v[4:7]\n\t" \
  "ds_read_b64_tr_b16 v[84:85], v160 offset:12288\n\t" \
  "s_waitcnt vmcnt(2)\n\t" \
  "v_mfma_f32_16x16x32_bf16 v[8:11], v[80:83], v[120:123], v[8:11]\n\t" \
  "ds_read_b64_tr_b16 v[86:87], v160 offset:12544\n\t" \
  "v_xor_b32 v188, s28, v241\n\t" \
  "v_mfma_f32_16x16x32_bf16 v[12:15], v[80:83], v[124:127], v[12:15]\n\t" \
  "ds_read_b128 v[132:135], v166 offset:2048\n\t" \
  "s_waitcnt lgkmcnt(4)\n\t" \
  "v_mfma_f32_16x16x32_bf16 v[16:19], v[88:91], v[112:115], v[16:19]\n\t" \
  "ds_read_b128 v[136:139], v166 offset:4096\n\t" \
  "v_xor_b32 v189, s28, v243\n\t" \
  "v_mfma_f32_16x16x32_bf16 v[20:23], v[88:91], v[116:119], v[20:23]\n\t" \
  "ds_read_b128 v[140:143], v166 offset:6144\n\t" \
  "v_bitop3_b32 v188, v188, s28, v245 bitop3:0xf6\n\t" \
  "v_mfma_f32_16x16x32_bf16 v[24:27], v[88:91], v[120:123], v[24:27]\n\t" \
  "ds_read_b64_tr_b16 v[92:93], v161 offset:12288\n\t" \
  "v_bitop3_b32 v189, v189, s28, v247 bitop3:0xf6\n\t" \
  "v_mfma_f32_16x16x32_bf16 v[28:31], v[88:91], v[124:127], v[28:31]\n\t" \
  "ds_read_b64_tr_b16 v[94:95], v161 offset:12544\n\t" \
  "v_bitop3_b32 v188, v188, s28, v249 bitop3:0xf6\n\t" \
  "v_mfma_f32_16x16x32_bf16 v[32:35], v[96:99], v[112:115], v[32:35]\n\t" \
  "ds_read_b64_tr_b16 v[100:101], v162 offset:12288\n\t" \
  "v_bitop3_b32 v189, v189, s28, v251 bitop3:0xf6\n\t" \
  "v_mfma_f32_16x16x32_bf16 v[36:39], v[96:99], v[116:119], v[36:39]\n\t" \
  "ds_read_b64_tr_b16 v[102:103], v162 offset:12544\n\t" \
  "v_bitop3_b32 v188, v188, s28, v253 bitop3:0xf6\n\t" \
  "v_mfma_f32_16x16x32_bf16 v[40:43], v[96:99], v[120:123], v[40:43]\n\t" \
  "v_bitop3_b32 v189, v189, s28, v255 bitop3:0xf6\n\t" \
  "v_or_b32 v188, v188, v189\n\t" \
  "v_mfma_f32_16x16x32_bf16 v[44:47], v[96:99], v[124:127], v[44:47]\n\t" \
  "v_cmp_ne_u32 vcc, 0, v188\n\t" \
  "s_waitcnt lgkmcnt(4)\n\t" \
  "v_smfmac_f32_16x16x64_bf16 v[72:75], v[176:179], v[80:87], v174\n\t" \
  "s_mul_i32 s63, s63, 3072\n\t" \
  "v_mfma_f32_16x16x32_bf16 v[0:3], v[84:87], v[128:131], v[0:3]\n\t" \
  "s_mul_i32 s64, s71, 81920\n\t" \
  "v_mfma_f32_16x16x32_bf16 v[4:7], v[84:87], v[132:135], v[4:7]\n\t" \
  "s_cbranch_vccnz .Lstqa%=\n\t" \
  ".Lstqb%=:\n\t" \
  "v_mfma_f32_16x16x32_bf16 v[8:11], v[84:87], v[136:139], v[8:11]\n\t" \
  "s_setprio 1\n\t" \
  "s_waitcnt lgkmcnt(0)\n\t" \
  "s_barrier\n\t" \
  "v_smfmac_f32_16x16x64_bf16 v[76:79], v[176:179], v[88:95], v174\n\t" \
  "v_mul_f32 v242, s41, v242\n\t" \
  "v_mfma_f32_16x16x32_bf16 v[12:15], v[84:87], v[140:143], v[12:15]\n\t" \
  "v_mul_f32 v246, s41, v246\n\t" \
  "v_mul_f32 v250, s41, v250\n\t" \
  "v_mfma_f32_16x16x32_bf16 v[16:19], v[92:95], v[128:131], v[16:19]\n\t" \
  "v_mul_f32 v254, s41, v254\n\t" \
  "s_add_u32 s63, s63, s64\n\t" \
  "v_mfma_f32_16x16x32_bf16 v[20:23], v[92:95], v[132:135], v[20:23]\n\t" \
  "s_add_u32 s63, s63, 819200\n\t" \
  "v_add_f32 v72, v72, v73\n\t" \
  "v_mfma_f32_16x16x32_bf16 v[24:27], v[92:95], v[136:139], v[24:27]\n\t" \
  "v_add_f32 v76, v76, v77\n\t" \
  "v_mov_b32 v73, 0\n\t" \
  "v_mfma_f32_16x16x32_bf16 v[28:31], v[92:95], v[140:143], v[28:31]\n\t" \
  "s_mov_b64 exec, 0xffff\n\t" \
  "ds_write_b32 v173, v72 offset:0\n\t" \
  "ds_write_b32 v184, v76 offset:0\n\t" \
  "ds_write_b32 v192, v73 offset:768\n\t" \
  "s_mov_b64 exec, -1\n\t" \
  "v_mfma_f32_16x16x32_bf16 v[32:35], v[100:103], v[128:131], v[32:35]\n\t" \
  "s_add_u32 s36, s24, s63\n\t" \
  "s_addc_u32 s37, s25, 0\n\t" \
  "v_mfma_f32_16x16x32_bf16 v[36:39], v[100:103], v[132:135], v[36:39]\n\t" \
  "s_add_u32 s38, s36, 40960\n\t" \
  "s_addc_u32 s39, s37, 0\n\t" \
  "v_mfma_f32_16x16x32_bf16 v[40:43], v[100:103], v[136:139], v[40:43]\n\t" \
  "v_mfma_f32_16x16x32_bf16 v[44:47], v[100:103], v[140:143], v[44:47]\n\t" \
  "s_setprio 0\n\t" \
  "s_waitcnt lgkmcnt(0)\n\t" \
  "s_barrier\n\t" \
  "ds_read_b128 v[96:99], v175 offset:0\n\t" \
  "ds_read_b128 v[100:103], v232 offset:0\n\t" \
  "ds_read_b128 v[104:107], v172 offset:0\n\t" \
  "s_cmp_ge_u32 s42, 14\n\t" \
  "s_cbranch_scc1 .Lkv%=\n\t" \
  "s_waitcnt lgkmcnt(2)\n\t" \
  "v_pk_fma_f32 v[0:1], v[240:241], v[96:97], v[0:1] op_sel_hi:[0,1,1] neg_lo:[1,0,0] neg_hi:[1,0,0]\n\t" \
  "v_pk_fma_f32 v[2:3], v[240:241], v[98:99], v[2:3] op_sel_hi:[0,1,1] neg_lo:[1,0,0] neg_hi:[1,0,0]\n\t" \
  "v_pk_fma_f32 v[4:5], v[244:245], v[96:97], v[4:5] op_sel_hi:[0,1,1] neg_lo:[1,0,0] neg_hi:[1,0,0]\n\t" \
  "v_pk_fma_f32 v[6:7], v[244:245], v[98:99], v[6:7] op_sel_hi:[0,1,1] neg_lo:[1,0,0] neg_hi:[1,0,0]\n\t" \
  "v_pk_mul_f32 v[0:1], v[242:243], v[0:1] op_sel_hi:[0,1]\n\t" \
  "v_pk_mul_f32 v[2:3], v[242:243], v[2:3] op_sel_hi:[0,1]\n\t" \
  "v_pk_mul_f32 v[4:5], v[246:247], v[4:5] op_sel_hi:[0,1]\n\t" \
  "v_pk_mul_f32 v[6:7], v[246:247], v[6:7] op_sel_hi:[0,1]\n\t" \
  "v_cvt_pk_bf16_f32 v0, v0, v1\n\t" \
  "v_cvt_pk_bf16_f32 v1, v2, v3\n\t" \
  "v_cvt_pk_bf16_f32 v2, v4, v5\n\t" \
  "v_cvt_pk_bf16_f32 v3, v6, v7\n\t" \
  "global_store_dwordx4 v196, v[0:3], s[36:37] sc1\n\t" \
  "v_pk_fma_f32 v[8:9], v[248:249], v[96:97], v[8:9] op_sel_hi:[0,1,1] neg_lo:[1,0,0] neg_hi:[1,0,0]\n\t" \
  "v_pk_fma_f32 v[10:11], v[248:249], v[98:99], v[10:11] op_sel_hi:[0,1,1] neg_lo:[1,0,0] neg_hi:[1,0,0]\n\t" \
  "v_pk_fma_f32 v[12:13], v[252:253], v[96:97], v[12:13] op_sel_hi:[0,1,1] neg_lo:[1,0,0] neg_hi:[1,0,0]\n\t" \
  "v_pk_fma_f32 v[14:15], v[252:253], v[98:99], v[14:15] op_sel_hi:[0,1,1] neg_lo:[1,0,0] neg_hi:[1,0,0]\n\t" \
  "v_pk_mul_f32 v[8:9], v[250:251], v[8:9] op_sel_hi:[0,1]\n\t" \
  "v_pk_mul_f32 v[10:11], v[250:251], v[10:11] op_sel_hi:[0,1]\n\t" \
  "v_pk_mul_f32 v[12:13], v[254:255], v[12:13] op_sel_hi:[0,1]\n\t" \
  "v_pk_mul_f32 v[14:15], v[254:255], v[14:15] op_sel_hi:[0,1]\n\t" \
  "v_cvt_pk_bf16_f32 v8, v8, v9\n\t" \
  "v_cvt_pk_bf16_f32 v9, v10, v11\n\t" \
  "v_cvt_pk_bf16_f32 v10, v12, v13\n\t" \
  "v_cvt_pk_bf16_f32 v11, v14, v15\n\t" \
  "global_store_dwordx4 v196, v[8:11], s[38:39] sc1\n\t" \
  "s_waitcnt lgkmcnt(1)\n\t" \
  "v_pk_fma_f32 v[16:17], v[240:241], v[100:101], v[16:17] op_sel_hi:[0,1,1] neg_lo:[1,0,0] neg_hi:[1,0,0]\n\t" \
  "v_pk_fma_f32 v[18:19], v[240:241], v[102:103], v[18:19] op_sel_hi:[0,1,1] neg_lo:[1,0,0] neg_hi:[1,0,0]\n\t" \
  "v_pk_fma_f32 v[20:21], v[244:245], v[100:101], v[20:21] op_sel_hi:[0,1,1] neg_lo:[1,0,0] neg_hi:[1,0,0]\n\t" \
  "v_pk_fma_f32 v[22:23], v[244:245], v[102:103], v[22:23] op_sel_hi:[0,1,1] neg_lo:[1,0,0] neg_hi:[1,0,0]\n\t" \
  "v_pk_mul_f32 v[16:17], v[242:243], v[16:17] op_sel_hi:[0,1]\n\t" \
  "v_pk_mul_f32 v[18:19], v[242:243], v[18:19] op_sel_hi:[0,1]\n\t" \
  "v_pk_mul_f32 v[20:21], v[246:247], v[20:21] op_sel_hi:[0,1]\n\t" \
  "v_pk_mul_f32 v[22:23], v[246:247], v[22:23] op_sel_hi:[0,1]\n\t" \
  "v_cvt_pk_bf16_f32 v16, v16, v17\n\t" \
  "v_cvt_pk_bf16_f32 v17, v18, v19\n\t" \
  "v_cvt_pk_bf16_f32 v18, v20, v21\n\t" \
  "v_cvt_pk_bf16_f32 v19, v22, v23\n\t" \
  "global_store_dwordx4 v197, v[16:19], s[36:37] sc1\n\t" \
  "v_pk_fma_f32 v[24:25], v[248:249], v[100:101], v[24:25] op_sel_hi:[0,1,1] neg_lo:[1,0,0] neg_hi:[1,0,0]\n\t" \
  "v_pk_fma_f32 v[26:27], v[248:249], v[102:103], v[26:27] op_sel_hi:[0,1,1] neg_lo:[1,0,0] neg_hi:[1,0,0]\n\t" \
  "v_pk_fma_f32 v[28:29], v[252:253], v[100:101], v[28:29] op_sel_hi:[0,1,1] neg_lo:[1,0,0] neg_hi:[1,0,0]\n\t" \
  "v_pk_fma_f32 v[30:31], v[252:253], v[102:103], v[30:31] op_sel_hi:[0,1,1] neg_lo:[1,0,0] neg_hi:[1,0,0]\n\t" \
  "v_pk_mul_f32 v[24:25], v[250:251], v[24:25] op_sel_hi:[0,1]\n\t" \
  "v_pk_mul_f32 v[26:27], v[250:251], v[26:27] op_sel_hi:[0,1]\n\t" \
  "v_pk_mul_f32 v[28:29], v[254:255], v[28:29] op_sel_hi:[0,1]\n\t" \
  "v_pk_mul_f32 v[30:31], v[254:255], v[30:31] op_sel_hi:[0,1]\n\t" \
  "v_cvt_pk_bf16_f32 v24, v24, v25\n\t" \
  "v_cvt_pk_bf16_f32 v25, v26, v27\n\t" \
  "v_cvt_pk_bf16_f32 v26, v28, v29\n\t" \
  "v_cvt_pk_bf16_f32 v27, v30, v31\n\t" \
  "global_store_dwordx4 v197, v[24:27], s[38:39] sc1\n\t" \
  "s_waitcnt lgkmcnt(0)\n\t" \
  "s_cmp_ge_u32 s42, 12\n\t" \
  "s_cbranch_scc1 .Lqd%=\n\t" \
  "v_pk_fma_f32 v[32:33], v[240:241], v[104:105], v[32:33] op_sel_hi:[0,1,1] neg_lo:[1,0,0] neg_hi:[1,0,0]\n\t" \
  "v_pk_fma_f32 v[34:35], v[240:241], v[106:107], v[34:35] op_sel_hi:[0,1,1] neg_lo:[1,0,0] neg_hi:[1,0,0]\n\t" \
  "v_pk_fma_f32 v[36:37], v[244:245], v[104:105], v[36:37] op_sel_hi:[0,1,1] neg_lo:[1,0,0] neg_hi:[1,0,0]\n\t" \
  "v_pk_fma_f32 v[38:39], v[244:245], v[106:107], v[38:39] op_sel_hi:[0,1,1] neg_lo:[1,0,0] neg_hi:[1,0,0]\n\t" \
  "v_pk_mul_f32 v[32:33], v[242:243], v[32:33] op_sel_hi:[0,1]\n\t" \
  "v_pk_mul_f32 v[34:35], v[242:243], v[34:35] op_sel_hi:[0,1]\n\t" \
  "v_pk_mul_f32 v[36:37], v[246:247], v[36:37] op_sel_hi:[0,1]\n\t" \
  "v_pk_mul_f32 v[38:39], v[246:247], v[38:39] op_sel_hi:[0,1]\n\t" \
  "v_cvt_pk_bf16_f32 v32, v32, v33\n\t" \
  "v_cvt_pk_bf16_f32 v33, v34, v35\n\t" \
  "v_cvt_pk_bf16_f32 v34, v36, v37\n\t" \
  "v_cvt_pk_bf16_f32 v35, v38, v39\n\t" \
  "global_store_dwordx4 v198, v[32:35], s[36:37] sc1\n\t" \
  "v_pk_fma_f32 v[40:41], v[248:249], v[104:105], v[40:41] op_sel_hi:[0,1,1] neg_lo:[1,0,0] neg_hi:[1,0,0]\n\t" \
  "v_pk_fma_f32 v[42:43], v[248:249], v[106:107], v[42:43] op_sel_hi:[0,1,1] neg_lo:[1,0,0] neg_hi:[1,0,0]\n\t" \
  "v_pk_fma_f32 v[44:45], v[252:253], v[104:105], v[44:45] op_sel_hi:[0,1,1] neg_lo:[1,0,0] neg_hi:[1,0,0]\n\t" \
  "v_pk_fma_f32 v[46:47], v[252:253], v[106:107], v[46:47] op_sel_hi:[0,1,1] neg_lo:[1,0,0] neg_hi:[1,0,0]\n\t" \
  "v_pk_mul_f32 v[40:41], v[250:251], v[40:41] op_sel_hi:[0,1]\n\t" \
  "v_pk_mul_f32 v[42:43], v[250:251], v[42:43] op_sel_hi:[0,1]\n\t" \
  "v_pk_mul_f32 v[44:45], v[254:255], v[44:45] op_sel_hi:[0,1]\n\t" \
  "v_pk_mul_f32 v[46:47], v[254:255], v[46:47] op_sel_hi:[0,1]\n\t" \
  "v_cvt_pk_bf16_f32 v40, v40, v41\n\t" \
  "v_cvt_pk_bf16_f32 v41, v42, v43\n\t" \
  "v_cvt_pk_bf16_f32 v42, v44, v45\n\t" \
  "v_cvt_pk_bf16_f32 v43, v46, v47\n\t" \
  "global_store_dwordx4 v198, v[40:43], s[38:39] sc1\n\t" \
  "s_branch .Lqd%=\n\t" \
  ".Lkv%=:\n\t" \
  "s_mul_i32 s40, s32, 5120\n\t" \
  "s_add_u32 s40, s40, 1024\n\t" \
  "v_and_b32 v199, 0xf0, v237\n\t" \
  "v_mul_u32_u24 v199, 10, v199\n\t" \
  "v_lshrrev_b32 v195, 1, v229\n\t" \
  "v_add3_u32 v199, v199, v195, s40\n\t" \
  "s_mul_i32 s63, s71, 7168\n\t" \
  "s_add_u32 s63, s63, 591872\n\t" \
  "s_add_u32 s66, s24, s63\n\t" \
  "s_addc_u32 s67, s25, 0\n\t" \
  "s_waitcnt lgkmcnt(0)\n\t" \
  "v_pk_fma_f32 v[0:1], v[240:241], v[96:97], v[0:1] op_sel_hi:[0,1,1] neg_lo:[1,0,0] neg_hi:[1,0,0]\n\t" \
  "v_pk_fma_f32 v[2:3], v[240:241], v[98:99], v[2:3] op_sel_hi:[0,1,1] neg_lo:[1,0,0] neg_hi:[1,0,0]\n\t" \
  "v_pk_fma_f32 v[4:5], v[244:245], v[96:97], v[4:5] op_sel_hi:[0,1,1] neg_lo:[1,0,0] neg_hi:[1,0,0]\n\t" \
  "v_pk_fma_f32 v[6:7], v[244:245], v[98:99], v[6:7] op_sel_hi:[0,1,1] neg_lo:[1,0,0] neg_hi:[1,0,0]\n\t" \
  "v_pk_mul_f32 v[0:1], v[242:243], v[0:1] op_sel_hi:[0,1]\n\t" \
  "v_pk_mul_f32 v[2:3], v[242:243], v[2:3] op_sel_hi:[0,1]\n\t" \
  "v_pk_mul_f32 v[4:5], v[246:247], v[4:5] op_sel_hi:[0,1]\n\t" \
  "v_pk_mul_f32 v[6:7], v[246:247], v[6:7] op_sel_hi:[0,1]\n\t" \
  "v_cvt_pk_bf16_f32 v0, v0, v1\n\t" \
  "v_cvt_pk_bf16_f32 v1, v2, v3\n\t" \
  "v_cvt_pk_bf16_f32 v2, v4, v5\n\t" \
  "v_cvt_pk_bf16_f32 v3, v6, v7\n\t" \
  "ds_write_b64 v199, v[0:1] offset:0\n\t" \
  "ds_write_b64 v199, v[2:3] offset:80\n\t" \
  "v_pk_fma_f32 v[8:9], v[248:249], v[96:97], v[8:9] op_sel_hi:[0,1,1] neg_lo:[1,0,0] neg_hi:[1,0,0]\n\t" \
  "v_pk_fma_f32 v[10:11], v[248:249], v[98:99], v[10:11] op_sel_hi:[0,1,1] neg_lo:[1,0,0] neg_hi:[1,0,0]\n\t" \
  "v_pk_fma_f32 v[12:13], v[252:253], v[96:97], v[12:13] op_sel_hi:[0,1,1] neg_lo:[1,0,0] neg_hi:[1,0,0]\n\t" \
  "v_pk_fma_f32 v[14:15], v[252:253], v[98:99], v[14:15] op_sel_hi:[0,1,1] neg_lo:[1,0,0] neg_hi:[1,0,0]\n\t" \
  "v_pk_mul_f32 v[8:9], v[250:251], v[8:9] op_sel_hi:[0,1]\n\t" \
  "v_pk_mul_f32 v[10:11], v[250:251], v[10:11] op_sel_hi:[0,1]\n\t" \
  "v_pk_mul_f32 v[12:13], v[254:255], v[12:13] op_sel_hi:[0,1]\n\t" \
  "v_pk_mul_f32 v[14:15], v[254:255], v[14:15] op_sel_hi:[0,1]\n\t" \
  "v_cvt_pk_bf16_f32 v8, v8, v9\n\t" \
  "v_cvt_pk_bf16_f32 v9, v10, v11\n\t" \
  "v_cvt_pk_bf16_f32 v10, v12, v13\n\t" \
  "v_cvt_pk_bf16_f32 v11, v14, v15\n\t" \
  "ds_write_b64 v199, v[8:9] offset:2560\n\t" \
  "ds_write_b64 v199, v[10:11] offset:2640\n\t" \
  "v_pk_fma_f32 v[16:17], v[240:241], v[100:101], v[16:17] op_sel_hi:[0,1,1] neg_lo:[1,0,0] neg_hi:[1,0,0]\n\t" \
  "v_pk_fma_f32 v[18:19], v[240:241], v[102:103], v[18:19] op_sel_hi:[0,1,1] neg_lo:[1,0,0] neg_hi:[1,0,0]\n\t" \
  "v_pk_fma_f32 v[20:21], v[244:245], v[100:101], v[20:21] op_sel_hi:[0,1,1] neg_lo:[1,0,0] neg_hi:[1,0,0]\n\t" \
  "v_pk_fma_f32 v[22:23], v[244:245], v[102:103], v[22:23] op_sel_hi:[0,1,1] neg_lo:[1,0,0] neg_hi:[1,0,0]\n\t" \
  "v_pk_mul_f32 v[16:17], v[242:243], v[16:17] op_sel_hi:[0,1]\n\t" \
  "v_pk_mul_f32 v[18:19], v[242:243], v[18:19] op_sel_hi:[0,1]\n\t" \
  "v_pk_mul_f32 v[20:21], v[246:247], v[20:21] op_sel_hi:[0,1]\n\t" \
  "v_pk_mul_f32 v[22:23], v[246:247], v[22:23] op_sel_hi:[0,1]\n\t" \
  "v_cvt_pk_bf16_f32 v16, v16, v17\n\t" \
  "v_cvt_pk_bf16_f32 v17, v18, v19\n\t" \
  "v_cvt_pk_bf16_f32 v18, v20, v21\n\t" \
  "v_cvt_pk_bf16_f32 v19, v22, v23\n\t" \
  "ds_write_b64 v199, v[16:17] offset:32\n\t" \
  "ds_write_b64 v199, v[18:19] offset:112\n\t" \
  "v_pk_fma_f32 v[24:25], v[248:249], v[100:101], v[24:25] op_sel_hi:[0,1,1] neg_lo:[1,0,0] neg_hi:[1,0,0]\n\t" \
  "v_pk_fma_f32 v[26:27], v[248:249], v[102:103], v[26:27] op_sel_hi:[0,1,1] neg_lo:[1,0,0] neg_hi:[1,0,0]\n\t" \
  "v_pk_fma_f32 v[28:29], v[252:253], v[100:101], v[28:29] op_sel_hi:[0,1,1] neg_lo:[1,0,0] neg_hi:[1,0,0]\n\t" \
  "v_pk_fma_f32 v[30:31], v[252:253], v[102:103], v[30:31] op_sel_hi:[0,1,1] neg_lo:[1,0,0] neg_hi:[1,0,0]\n\t" \
  "v_pk_mul_f32 v[24:25], v[250:251], v[24:25] op_sel_hi:[0,1]\n\t" \
  "v_pk_mul_f32 v[26:27], v[250:251], v[26:27] op_sel_hi:[0,1]\n\t" \
  "v_pk_mul_f32 v[28:29], v[254:255], v[28:29] op_sel_hi:[0,1]\n\t" \
  "v_pk_mul_f32 v[30:31], v[254:255], v[30:31] op_sel_hi:[0,1]\n\t" \
  "v_cvt_pk_bf16_f32 v24, v24, v25\n\t" \
  "v_cvt_pk_bf16_f32 v25, v26, v27\n\t" \
  "v_cvt_pk_bf16_f32 v26, v28, v29\n\t" \
  "v_cvt_pk_bf16_f32 v27, v30, v31\n\t" \
  "ds_write_b64 v199, v[24:25] offset:2592\n\t" \
  "ds_write_b64 v199, v[26:27] offset:2672\n\t" \
  "s_cmp_eq_u32 s42, 14\n\t" \
  "s_cbranch_scc0 .Lv%=\n\t" \
  "v_and_b32 v82, 31, v228\n\t" \
  "v_mul_u32_u24 v82, 80, v82\n\t" \
  "v_add_u32 v82, s40, v82\n\t" \
  "v_lshrrev_b32 v83, 5, v228\n\t" \
  "v_lshl_add_u32 v83, v83, 4, v82\n\t" \
  "v_lshrrev_b32 v84, 1, v237\n\t" \
  "v_add_u32 v85, 3584, v84\n\t" \
  "ds_read_b128 v[32:35], v83 offset:0\n\t" \
  "ds_read_b64 v[40:41], v82 offset:40\n\t" \
  "ds_read_b128 v[36:39], v83 offset:2560\n\t" \
  "ds_read_b64 v[42:43], v82 offset:2600\n\t" \
  "s_waitcnt lgkmcnt(0)\n\t" \
  "s_mov_b32 exec_lo, 0\n\t" \
  "v_mov_b32 v40, 0x3f803f80\n\t" \
  "v_mov_b32 v41, 0x3f80\n\t" \
  "v_mov_b64 v[42:43], v[40:41]\n\t" \
  "s_mov_b32 exec_lo, -1\n\t" \
  "global_store_dwordx4 v237, v[32:35], s[66:67] offset:-2048 sc1\n\t" \
  "global_store_dwordx4 v237, v[36:39], s[66:67] offset:1536 sc1\n\t" \
  "global_store_dwordx2 v84, v[40:41], s[66:67] offset:1024 sc1\n\t" \
  "global_store_dwordx2 v85, v[42:43], s[66:67] offset:1024 sc1\n\t" \
  "s_branch .Lqd%=\n\t" \
  ".Lv%=:\n\t" \
  "v_bfe_u32 v82, v228, 2, 2\n\t" \
  "v_lshrrev_b32 v83, 5, v228\n\t" \
  "v_lshl_or_b32 v82, v83, 3, v82\n\t" \
  "v_mul_u32_u24 v82, 80, v82\n\t" \
  "v_and_b32 v83, 3, v228\n\t" \
  "v_lshl_add_u32 v82, v83, 3, v82\n\t" \
  "v_bfe_u32 v83, v228, 4, 1\n\t" \
  "v_mad_u32_u24 v82, v83, 40, v82\n\t" \
  "v_add_u32 v82, s40, v82\n\t" \
  "ds_read_b64_tr_b16 v[32:33], v82 offset:0\n\t" \
  "ds_read_b64_tr_b16 v[34:35], v82 offset:320\n\t" \
  "ds_read_b64_tr_b16 v[36:37], v82 offset:1280\n\t" \
  "ds_read_b64_tr_b16 v[38:39], v82 offset:1600\n\t" \
  "ds_read_b64_tr_b16 v[40:41], v82 offset:2560\n\t" \
  "ds_read_b64_tr_b16 v[42:43], v82 offset:2880\n\t" \
  "ds_read_b64_tr_b16 v[44:45], v82 offset:3840\n\t" \
  "ds_read_b64_tr_b16 v[46:47], v82 offset:4160\n\t" \
  "v_and_b32 v83, 31, v228\n\t" \
  "v_cmp_eq_u32 vcc, 20, v83\n\t" \
  "v_mov_b32 v84, 0x3f803f80\n\t" \
  "s_nop 0\n\t" \
  "v_cndmask_b32 v84, 0, v84, vcc\n\t" \
  "v_mov_b32 v85, v84\n\t" \
  "s_waitcnt lgkmcnt(0)\n\t" \
  "s_mov_b32 exec_lo, 0xfff00000\n\t" \
  "s_mov_b32 exec_hi, 0xfff00000\n\t" \
  "v_mov_b64 v[32:33], v[84:85]\n\t" \
  "v_mov_b64 v[34:35], v[84:85]\n\t" \
  "v_mov_b64 v[36:37], v[84:85]\n\t" \
  "v_mov_b64 v[38:39], v[84:85]\n\t" \
  "v_mov_b64 v[40:41], v[84:85]\n\t" \
  "v_mov_b64 v[42:43], v[84:85]\n\t" \
  "v_mov_b64 v[44:45], v[84:85]\n\t" \
  "v_mov_b64 v[46:47], v[84:85]\n\t" \
  "s_mov_b64 exec, -1\n\t" \
  "global_store_dwordx4 v237, v[32:35], s[66:67] offset:-1024 sc1\n\t" \
  "global_store_dwordx4 v237, v[36:39], s[66:67] offset:0 sc1\n\t" \
  "global_store_dwordx4 v237, v[40:43], s[66:67] offset:2560 sc1\n\t" \
  "global_store_dwordx4 v237, v[44:47], s[66:67] offset:3584 sc1\n\t" \
  ".Lqd%=:\n\t" \
  "v_mov_b64 v[152:153], s[28:29]\n\t" \
  "v_mov_b64 v[154:155], s[28:29]\n\t" \
  "v_add_u32 v156, 262144, v231\n\t" \
  "s_waitcnt vmcnt(0)\n\t" \
  "s_barrier\n\t" \
  "s_cmp_eq_u32 s32, 4\n\t" \
  "s_cbranch_scc0 .Lqp%=\n\t" \
  "global_store_dwordx4 v231, v[152:155], s[24:25] sc1\n\t" \
  "global_store_dwordx4 v156, v[152:155], s[24:25] sc1\n\t" \
  ".Lqp%=:\n\t" \
  "s_cmp_lt_u32 s32, 4\n\t" \
  "s_cbranch_scc0 .Lqg%=\n\t" \
  "s_mul_i32 s63, s32, 57344\n\t" \
  "s_add_u32 s63, s63, 589824\n\t" \
  "s_add_u32 s36, s24, s63\n\t" \
  "s_addc_u32 s37, s25, 0\n\t" \
  "v_lshrrev_b32 v238, 1, v237\n\t" \
  "s_lshl2_add_u32 s63, s62, s32\n\t" \
  "s_mul_i32 s63, s63, 2560\n\t" \
  "s_add_u32 s63, s63, 819200\n\t" \
  "s_add_u32 s38, s24, s63\n\t" \
  "s_addc_u32 s39, s25, 0\n\t" \
  "v_mov_b64 v[212:213], 0\n\t" \
  "v_and_b32 v214, 31, v228\n\t" \
  "v_lshrrev_b32 v215, 5, v228\n\t" \
  "v_mul_u32_u24 v239, 336, v214\n\t" \
  "v_lshl_add_u32 v239, v215, 7, v239\n\t" \
  "v_add_u32 v239, 65536, v239\n\t" \
  "v_mul_u32_u24 v214, 80, v214\n\t" \
  "s_mul_i32 s63, s32, 2560\n\t" \
  "s_add_u32 s64, s63, 21504\n\t" \
  "v_lshl_add_u32 v215, v215, 4, s64\n\t" \
  "v_mov_b32 v236, 0xdddddddd\n\t" \
  "s_mul_i32 s63, s32, 2688\n\t" \
  "s_add_u32 s63, s63, 65536\n\t" \
  "v_mul_u32_u24 v218, 13, v228\n\t" \
  "v_lshrrev_b32 v218, 8, v218\n\t" \
  "v_add_u32 v218, v218, v228\n\t" \
  "v_lshl_add_u32 v218, v218, 4, s63\n\t" \
  "v_and_b32 v195, 3, v228\n\t" \
  "v_mul_u32_u24 v194, 10304, v195\n\t" \
  "v_add_u32 v194, 62720, v194\n\t" \
  "v_mul_u32_u24 v195, 2080, v195\n\t" \
  "v_add_u32 v195, 112256, v195\n\t" \
  "v_add_u32 v241, v214, v215\n\t" \
  "v_add_u32 v243, 41216, v241\n\t" \
  "v_mov_b32 v245, 804\n\t" \
  "v_mov_b32 v247, 1\n\t" \
  "v_bfe_u32 v196, v228, 2, 2\n\t" \
  "v_lshrrev_b32 v197, 4, v228\n\t" \
  "v_lshl_or_b32 v196, v197, 3, v196\n\t" \
  "s_lshl_b32 s65, s32, 5\n\t" \
  "v_add_u32 v197, s65, v196\n\t" \
  "v_lshl_add_u32 v249, v197, 4, v194\n\t" \
  "v_mul_u32_u24 v197, 205, v197\n\t" \
  "v_lshrrev_b32 v251, 10, v197\n\t" \
  "v_add_u32 v197, 820, v197\n\t" \
  "v_lshrrev_b32 v253, 10, v197\n\t" \
  "v_lshl_add_u32 v251, v251, 4, v195\n\t" \
  "v_lshl_add_u32 v253, v253, 4, v195\n\t" \
  "s_add_u32 s30, s30, 262144\n\t" \
  "s_addc_u32 s31, s31, 0\n\t" \
  "v_mfma_f32_32x32x8_bf16 v[192:207], v[212:213], v[212:213], 0\n\t" \
  "s_branch .Lahead%=\n\t" \
  ".Lqg%=:\n\t" \
  "s_setprio 2\n\t" \
  "s_nop 9\n\t" \
  "v_mul_u32_u24 v194, 6554, v228\n\t" \
  "s_movk_i32 s64, 1120\n\t" \
  "v_lshrrev_b32 v195, 16, v194\n\t" \
  "v_mad_u32_u24 v220, v195, s64, v237\n\t" \
  "v_add_u32 v194, 419456, v194\n\t" \
  "v_lshrrev_b32 v195, 16, v194\n\t" \
  "v_mad_u32_u24 v221, v195, s64, v237\n\t" \
  "v_add_u32 v194, 419456, v194\n\t" \
  "v_lshrrev_b32 v195, 16, v194\n\t" \
  "v_mad_u32_u24 v222, v195, s64, v237\n\t" \
  "v_add_u32 v194, 419456, v194\n\t" \
  "v_lshrrev_b32 v195, 16, v194\n\t" \
  "v_mad_u32_u24 v223, v195, s64, v237\n\t" \
  "v_add_u32 v194, 419456, v194\n\t" \
  "v_lshrrev_b32 v195, 16, v194\n\t" \
  "v_mad_u32_u24 v224, v195, s64, v237\n\t" \
  "v_add_u32 v224, 4096, v224\n\t" \
  "s_mul_i32 s48, s34, 10240\n\t" \
  "s_add_u32 s48, s48, 122880\n\t" \
  "s_and_b32 s64, s62, 7\n\t" \
  "s_mul_i32 s64, s64, 160\n\t" \
  "s_mul_i32 s63, s34, 573440\n\t" \
  "s_add_u32 s63, s63, s64\n\t" \
  "s_add_u32 s36, s22, s63\n\t" \
  "s_addc_u32 s37, s23, 0\n\t" \
  "s_branch .Lgsetup%=\n\t" \
  ".Lano%=:\n\t" \
  "s_mov_b32 s42, 0\n\t" \
  "s_add_u32 s30, s30, 262144\n\t" \
  "s_addc_u32 s31, s31, 0\n\t" \
  "global_load_dwordx2 v[234:235], v233, s[30:31] sc1\n\t" \
  "s_branch .Layes%=\n\t" \
  ".Lstaa%=:\n\t" \
  "s_mov_b32 s54, 0x400000\n\t" \
  ".Lstac%=:\n\t" \
  "s_sleep 2\n\t" \
  "global_load_dwordx4 v[240:243], v180, s[46:47] sc1\n\t" \
  "global_load_dwordx4 v[244:247], v181, s[46:47] sc1\n\t" \
  "global_load_dwordx4 v[248:251], v182, s[46:47] sc1\n\t" \
  "global_load_dwordx4 v[252:255], v183, s[46:47] sc1\n\t" \
  "s_waitcnt vmcnt(0)\n\t" \
  "v_xor_b32 v188, s28, v241\n\t" \
  "v_xor_b32 v189, s28, v243\n\t" \
  "v_bitop3_b32 v188, v188, s28, v245 bitop3:0xf6\n\t" \
  "v_bitop3_b32 v189, v189, s28, v247 bitop3:0xf6\n\t" \
  "v_bitop3_b32 v188, v188, s28, v249 bitop3:0xf6\n\t" \
  "v_bitop3_b32 v189, v189, s28, v251 bitop3:0xf6\n\t" \
  "v_bitop3_b32 v188, v188, s28, v253 bitop3:0xf6\n\t" \
  "v_bitop3_b32 v189, v189, s28, v255 bitop3:0xf6\n\t" \
  "v_or_b32 v188, v188, v189\n\t" \
  "v_cmp_ne_u32 vcc, 0, v188\n\t" \
  "s_nop 1\n\t" \
  "s_cbranch_vccz .Lstab%=\n\t" \
  "s_sub_u32 s54, s54, 1\n\t" \
  "s_cbranch_scc0 .Lstac%=\n\t" \
  "s_branch .Lstab%=\n\t" \
  ".Laqw%=:\n\t" \
  "ds_read_b32 v247, v245 offset:8\n\t" \
  "s_waitcnt lgkmcnt(0)\n\t" \
  "s_branch .Lalast%=\n\t" \
  ".Lstga%=:\n\t" \
  "s_mov_b32 s54, 0x400000\n\t" \
  ".Lstgc%=:\n\t" \
  "s_sleep 2\n\t" \
  "global_load_dwordx4 v[240:243], v180, s[46:47] sc1\n\t" \
  "global_load_dwordx4 v[244:247], v181, s[46:47] sc1\n\t" \
  "global_load_dwordx4 v[248:251], v182, s[46:47] sc1\n\t" \
  "global_load_dwordx4 v[252:255], v183, s[46:47] sc1\n\t" \
  "s_waitcnt vmcnt(0)\n\t" \
  "v_xor_b32 v188, s28, v241\n\t" \
  "v_xor_b32 v189, s28, v243\n\t" \
  "v_bitop3_b32 v188, v188, s28, v245 bitop3:0xf6\n\t" \
  "v_bitop3_b32 v189, v189, s28, v247 bitop3:0xf6\n\t" \
  "v_bitop3_b32 v188, v188, s28, v249 bitop3:0xf6\n\t" \
  "v_bitop3_b32 v189, v189, s28, v251 bitop3:0xf6\n\t" \
  "v_bitop3_b32 v188, v188, s28, v253 bitop3:0xf6\n\t" \
  "v_bitop3_b32 v189, v189, s28, v255 bitop3:0xf6\n\t" \
  "v_or_b32 v188, v188, v189\n\t" \
  "v_cmp_ne_u32 vcc, 0, v188\n\t" \
  "s_nop 1\n\t" \
  "s_cbranch_vccz .Lstgb%=\n\t" \
  "s_sub_u32 s54, s54, 1\n\t" \
  "s_cbranch_scc0 .Lstgc%=\n\t" \
  "s_branch .Lstgb%=\n\t" \
  ".Lgq%=:\n\t" \
  "s_sleep 8\n\t" \
  "s_add_u32 m0, s48, 0\n\t" \
  "s_nop 0\n\t" \
  "global_load_lds_dwordx4 v220, s[36:37] offset:0\n\t" \
  "global_load_lds_dwordx4 v221, s[36:37] offset:1024\n\t" \
  "global_load_lds_dwordx4 v222, s[36:37] offset:2048\n\t" \
  "global_load_lds_dwordx4 v223, s[36:37] offset:3072\n\t" \
  "s_add_u32 m0, m0, 4096\n\t" \
  "s_nop 0\n\t" \
  "global_load_lds_dwordx4 v224, s[36:37]\n\t" \
  "s_add_u32 s36, s36, 40960\n\t" \
  "s_addc_u32 s37, s37, 0\n\t" \
  "s_waitcnt vmcnt(0)\n\t" \
  "s_add_u32 m0, s48, 5120\n\t" \
  "s_nop 0\n\t" \
  "global_load_lds_dwordx4 v220, s[36:37] offset:0\n\t" \
  "global_load_lds_dwordx4 v221, s[36:37] offset:1024\n\t" \
  "global_load_lds_dwordx4 v222, s[36:37] offset:2048\n\t" \
  "global_load_lds_dwordx4 v223, s[36:37] offset:3072\n\t" \
  "s_add_u32 m0, m0, 4096\n\t" \
  "s_nop 0\n\t" \
  "global_load_lds_dwordx4 v224, s[36:37]\n\t" \
  "s_add_u32 s36, s36, 40960\n\t" \
  "s_addc_u32 s37, s37, 0\n\t" \
  "s_waitcnt vmcnt(0)\n\t" \
  "s_branch .Lgnoh%=\n\t" \
  ".Lgr0%=:\n\t" \
  "s_sub_u32 s64, s64, 1\n\t" \
  "s_waitcnt vmcnt(0)\n\t" \
  "s_cbranch_scc1 .Lgx0%=\n\t" \
  "global_load_dwordx4 v[180:183], v227, s[46:47] offset:-3072 sc1\n\t" \
  "global_load_dwordx4 v[184:187], v227, s[46:47] offset:-2816 sc1\n\t" \
  "global_load_dwordx4 v[188:191], v227, s[46:47] offset:-2560 sc1\n\t" \
  "global_load_dwordx4 v[192:195], v227, s[46:47] offset:-2304 sc1\n\t" \
  "global_load_dwordx4 v[196:199], v227, s[46:47] offset:-2048 sc1\n\t" \
  "global_load_dwordx4 v[200:203], v236, s[46:47] offset:-3072 sc1\n\t" \
  "global_load_dwordx4 v[204:207], v236, s[46:47] offset:-2816 sc1\n\t" \
  "global_load_dwordx4 v[208:211], v236, s[46:47] offset:-2560 sc1\n\t" \
  "global_load_dwordx4 v[212:215], v236, s[46:47] offset:-2304 sc1\n\t" \
  "global_load_dwordx4 v[216:219], v236, s[46:47] offset:-2048 sc1\n\t" \
  "s_waitcnt vmcnt(5)\n\t" \
  "v_max3_u32 v254, v180, v181, v182\n\t" \
  "v_max3_u32 v255, v183, v184, v185\n\t" \
  "v_max3_u32 v254, v254, v186, v187\n\t" \
  "v_max3_u32 v255, v255, v188, v189\n\t" \
  "v_max3_u32 v254, v254, v190, v191\n\t" \
  "v_max3_u32 v255, v255, v192, v193\n\t" \
  "v_max3_u32 v254, v254, v194, v195\n\t" \
  "v_max3_u32 v255, v255, v196, v197\n\t" \
  "v_max3_u32 v254, v254, v198, v199\n\t" \
  "v_max_u32 v254, v254, v255\n\t" \
  "v_cmp_eq_u32 vcc, -1, v254\n\t" \
  "s_nop 1\n\t" \
  "s_cbranch_vccnz .Lgr0%=\n\t" \
  "s_branch .Lgm0%=\n\t" \
  ".Lgx0%=:\n\t" \
  "s_mov_b32 s64, 0\n\t" \
  "s_branch .Lgm0%=\n\t" \
  ".Lgr1%=:\n\t" \
  "s_sub_u32 s64, s64, 1\n\t" \
  "s_waitcnt vmcnt(0)\n\t" \
  "s_cbranch_scc1 .Lgx1%=\n\t" \
  "global_load_dwordx4 v[200:203], v236, s[46:47] offset:-3072 sc1\n\t" \
  "global_load_dwordx4 v[204:207], v236, s[46:47] offset:-2816 sc1\n\t" \
  "global_load_dwordx4 v[208:211], v236, s[46:47] offset:-2560 sc1\n\t" \
  "global_load_dwordx4 v[212:215], v236, s[46:47] offset:-2304 sc1\n\t" \
  "global_load_dwordx4 v[216:219], v236, s[46:47] offset:-2048 sc1\n\t" \
  "s_waitcnt vmcnt(0)\n\t" \
  "v_max3_u32 v254, v200, v201, v202\n\t" \
  "v_max3_u32 v255, v203, v204, v205\n\t" \
  "v_max3_u32 v254, v254, v206, v207\n\t" \
  "v_max3_u32 v255, v255, v208, v209\n\t" \
  "v_max3_u32 v254, v254, v210, v211\n\t" \
  "v_max3_u32 v255, v255, v212, v213\n\t" \
  "v_max3_u32 v254, v254, v214, v215\n\t" \
  "v_max3_u32 v255, v255, v216, v217\n\t" \
  "v_max3_u32 v254, v254, v218, v219\n\t" \
  "v_max_u32 v254, v254, v255\n\t" \
  "v_cmp_eq_u32 vcc, -1, v254\n\t" \
  "s_nop 1\n\t" \
  "s_cbranch_vccnz .Lgr1%=\n\t" \
  "s_branch .Lgm1%=\n\t" \
  ".Lgx1%=:\n\t" \
  "s_mov_b32 s64, 0\n\t" \
  "s_branch .Lgm1%=\n\t" \
  ".Lgr2%=:\n\t" \
  "s_sub_u32 s64, s64, 1\n\t" \
  "s_waitcnt vmcnt(0)\n\t" \
  "s_cbranch_scc1 .Lgx2%=\n\t" \
  "global_load_dwordx4 v[180:183], v227, s[46:47] offset:2048 sc1\n\t" \
  "global_load_dwordx4 v[184:187], v227, s[46:47] offset:2304 sc1\n\t" \
  "global_load_dwordx4 v[188:191], v227, s[46:47] offset:2560 sc1\n\t" \
  "global_load_dwordx4 v[192:195], v227, s[46:47] offset:2816 sc1\n\t" \
  "global_load_dwordx4 v[196:199], v227, s[46:47] offset:3072 sc1\n\t" \
  "global_load_dwordx4 v[200:203], v236, s[46:47] offset:2048 sc1\n\t" \
  "global_load_dwordx4 v[204:207], v236, s[46:47] offset:2304 sc1\n\t" \
  "global_load_dwordx4 v[208:211], v236, s[46:47] offset:2560 sc1\n\t" \
  "global_load_dwordx4 v[212:215], v236, s[46:47] offset:2816 sc1\n\t" \
  "global_load_dwordx4 v[216:219], v236, s[46:47] offset:3072 sc1\n\t" \
  "s_waitcnt vmcnt(5)\n\t" \
  "v_max3_u32 v254, v180, v181, v182\n\t" \
  "v_max3_u32 v255, v183, v184, v185\n\t" \
  "v_max3_u32 v254, v254, v186, v187\n\t" \
  "v_max3_u32 v255, v255, v188, v189\n\t" \
  "v_max3_u32 v254, v254, v190, v191\n\t" \
  "v_max3_u32 v255, v255, v192, v193\n\t" \
  "v_max3_u32 v254, v254, v194, v195\n\t" \
  "v_max3_u32 v255, v255, v196, v197\n\t" \
  "v_max3_u32 v254, v254, v198, v199\n\t" \
  "v_max_u32 v254, v254, v255\n\t" \
  "v_cmp_eq_u32 vcc, -1, v254\n\t" \
  "s_nop 1\n\t" \
  "s_cbranch_vccnz .Lgr2%=\n\t" \
  "s_branch .Lgm2%=\n\t" \
  ".Lgx2%=:\n\t" \
  "s_mov_b32 s64, 0\n\t" \
  "s_branch .Lgm2%=\n\t" \
  ".Lgr3%=:\n\t" \
  "s_sub_u32 s64, s64, 1\n\t" \
  "s_waitcnt vmcnt(0)\n\t" \
  "s_cbranch_scc1 .Lgx3%=\n\t" \
  "global_load_dwordx4 v[200:203], v236, s[46:47] offset:2048 sc1\n\t" \
  "global_load_dwordx4 v[204:207], v236, s[46:47] offset:2304 sc1\n\t" \
  "global_load_dwordx4 v[208:211], v236, s[46:47] offset:2560 sc1\n\t" \
  "global_load_dwordx4 v[212:215], v236, s[46:47] offset:2816 sc1\n\t" \
  "global_load_dwordx4 v[216:219], v236, s[46:47] offset:3072 sc1\n\t" \
  "s_waitcnt vmcnt(0)\n\t" \
  "v_max3_u32 v254, v200, v201, v202\n\t" \
  "v_max3_u32 v255, v203, v204, v205\n\t" \
  "v_max3_u32 v254, v254, v206, v207\n\t" \
  "v_max3_u32 v255, v255, v208, v209\n\t" \
  "v_max3_u32 v254, v254, v210, v211\n\t" \
  "v_max3_u32 v255, v255, v212, v213\n\t" \
  "v_max3_u32 v254, v254, v214, v215\n\t" \
  "v_max3_u32 v255, v255, v216, v217\n\t" \
  "v_max3_u32 v254, v254, v218, v219\n\t" \
  "v_max_u32 v254, v254, v255\n\t" \
  "v_cmp_eq_u32 vcc, -1, v254\n\t" \
  "s_nop 1\n\t" \
  "s_cbranch_vccnz .Lgr3%=\n\t" \
  "s_branch .Lgm3%=\n\t" \
  ".Lgx3%=:\n\t" \
  "s_mov_b32 s64, 0\n\t" \
  "s_branch .Lgm3%=\n\t" \
  ".Lg%=:\n\t" \
  "v_smfmac_f32_16x16x64_bf16 v[76:79], v[176:179], v[144:151], v174\n\t" \
  "ds_read_b128 v[112:115], v164 offset:0\n\t" \
  "v_mfma_f32_16x16x32_bf16 v[12:15], v[84:87], v[140:143], v[12:15]\n\t" \
  "ds_read_b64_tr_b16 v[80:81], v160 offset:0\n\t" \
  "s_add_u32 m0, s49, s56\n\t" \
  "v_mfma_f32_16x16x32_bf16 v[16:19], v[92:95], v[128:131], v[16:19]\n\t" \
  "ds_read_b64_tr_b16 v[82:83], v160 offset:256\n\t" \
  "global_load_lds_dwordx4 v170, s[38:39]\n\t" \
  "v_mfma_f32_16x16x32_bf16 v[20:23], v[92:95], v[132:135], v[20:23]\n\t" \
  "ds_read_b128 v[116:119], v165 offset:0\n\t" \
  "s_add_u32 m0, m0, 0x400\n\t" \
  "v_mfma_f32_16x16x32_bf16 v[24:27], v[92:95], v[136:139], v[24:27]\n\t" \
  "ds_read_b128 v[120:123], v164 offset:4096\n\t" \
  "global_load_lds_dwordx4 v171, s[38:39]\n\t" \
  "v_mfma_f32_16x16x32_bf16 v[28:31], v[92:95], v[140:143], v[28:31]\n\t" \
  "ds_read_b128 v[124:127], v165 offset:4096\n\t" \
  "s_add_u32 m0, s51, s56\n\t" \
  "v_mfma_f32_16x16x32_bf16 v[32:35], v[100:103], v[128:131], v[32:35]\n\t" \
  "ds_read_b64_tr_b16 v[88:89], v161 offset:0\n\t" \
  "s_mov_b32 exec_hi, 0\n\t" \
  "global_load_lds_dwordx4 v173, s[44:45]\n\t" \
  "s_mov_b32 exec_hi, -1\n\t" \
  "v_mfma_f32_16x16x32_bf16 v[36:39], v[100:103], v[132:135], v[36:39]\n\t" \
  "ds_read_b64_tr_b16 v[90:91], v161 offset:256\n\t" \
  "v_add_u32 v166, s55, v166\n\t" \
  "v_mfma_f32_16x16x32_bf16 v[40:43], v[100:103], v[136:139], v[40:43]\n\t" \
  "ds_read_b64_tr_b16 v[96:97], v162 offset:0\n\t" \
  "v_add_u32 v167, s55, v167\n\t" \
  "v_mfma_f32_16x16x32_bf16 v[44:47], v[100:103], v[140:143], v[44:47]\n\t" \
  "ds_read_b64_tr_b16 v[98:99], v162 offset:256\n\t" \
  "s_mov_b32 s55, s66\n\t" \
  "v_mfma_f32_16x16x32_bf16 v[48:51], v[108:111], v[128:131], v[48:51]\n\t" \
  "ds_read_b64_tr_b16 v[104:105], v163 offset:0\n\t" \
  "v_mul_u32_u24 v194, 6554, v228\n\t" \
  "v_mfma_f32_16x16x32_bf16 v[52:55], v[108:111], v[132:135], v[52:55]\n\t" \
  "ds_read_b64_tr_b16 v[106:107], v163 offset:256\n\t" \
  "s_movk_i32 s64, 1120\n\t" \
  "v_mfma_f32_16x16x32_bf16 v[56:59], v[108:111], v[136:139], v[56:59]\n\t" \
  "ds_read_b64_tr_b16 v[144:145], v168 offset:0\n\t" \
  "v_lshrrev_b32 v195, 16, v194\n\t" \
  "v_mfma_f32_16x16x32_bf16 v[60:63], v[108:111], v[140:143], v[60:63]\n\t" \
  "ds_read_b64_tr_b16 v[146:147], v168 offset:256\n\t" \
  "v_mad_u32_u24 v220, v195, s64, v237\n\t" \
  "v_mfma_f32_16x16x32_bf16 v[64:67], v[148:151], v[128:131], v[64:67]\n\t" \
  "ds_read_b64_tr_b16 v[152:153], v169 offset:0\n\t" \
  "v_mfma_f32_16x16x32_bf16 v[68:71], v[156:159], v[128:131], v[68:71]\n\t" \
  "ds_read_b64_tr_b16 v[154:155], v169 offset:256\n\t" \
  "s_waitcnt lgkmcnt(4)\n\t" \
  "v_mfma_f32_16x16x32_bf16 v[0:3], v[80:83], v[112:115], v[0:3]\n\t" \
  "ds_read_b128 v[128:131], v166 offset:0\n\t" \
  "s_setprio 0\n\t" \
  "v_mfma_f32_16x16x32_bf16 v[4:7], v[80:83], v[116:119], v[4:7]\n\t" \
  "ds_read_b64_tr_b16 v[84:85], v160 offset:8192\n\t" \
  "global_load_dwordx4 v[240:243], v180, s[46:47] sc1\n\t" \
  "v_mfma_f32_16x16x32_bf16 v[8:11], v[80:83], v[120:123], v[8:11]\n\t" \
  "ds_read_b64_tr_b16 v[86:87], v160 offset:8448\n\t" \
  "global_load_dwordx4 v[244:247], v181, s[46:47] sc1\n\t" \
  "v_mfma_f32_16x16x32_bf16 v[12:15], v[80:83], v[124:127], v[12:15]\n\t" \
  "ds_read_b128 v[132:135], v167 offset:0\n\t" \
  "s_waitcnt lgkmcnt(4)\n\t" \
  "v_mfma_f32_16x16x32_bf16 v[16:19], v[88:91], v[112:115], v[16:19]\n\t" \
  "ds_read_b128 v[136:139], v166 offset:4096\n\t" \
  "global_load_dwordx4 v[248:251], v182, s[46:47] sc1\n\t" \
  "v_mfma_f32_16x16x32_bf16 v[20:23], v[88:91], v[116:119], v[20:23]\n\t" \
  "ds_read_b128 v[140:143], v167 offset:4096\n\t" \
  "global_load_dwordx4 v[252:255], v183, s[46:47] sc1\n\t" \
  "v_mfma_f32_16x16x32_bf16 v[24:27], v[88:91], v[120:123], v[24:27]\n\t" \
  "ds_read_b64_tr_b16 v[92:93], v161 offset:8192\n\t" \
  "v_add_u32 v164, s55, v164\n\t" \
  "v_mfma_f32_16x16x32_bf16 v[28:31], v[88:91], v[124:127], v[28:31]\n\t" \
  "ds_read_b64_tr_b16 v[94:95], v161 offset:8448\n\t" \
  "v_add_u32 v165, s55, v165\n\t" \
  "v_mfma_f32_16x16x32_bf16 v[32:35], v[96:99], v[112:115], v[32:35]\n\t" \
  "ds_read_b64_tr_b16 v[100:101], v162 offset:8192\n\t" \
  "v_add_u32 v194, 419456, v194\n\t" \
  "v_mfma_f32_16x16x32_bf16 v[36:39], v[96:99], v[116:119], v[36:39]\n\t" \
  "ds_read_b64_tr_b16 v[102:103], v162 offset:8448\n\t" \
  "v_lshrrev_b32 v195, 16, v194\n\t" \
  "v_mfma_f32_16x16x32_bf16 v[40:43], v[96:99], v[120:123], v[40:43]\n\t" \
  "ds_read_b64_tr_b16 v[108:109], v163 offset:8192\n\t" \
  "v_mad_u32_u24 v221, v195, s64, v237\n\t" \
  "v_mfma_f32_16x16x32_bf16 v[44:47], v[96:99], v[124:127], v[44:47]\n\t" \
  "ds_read_b64_tr_b16 v[110:111], v163 offset:8448\n\t" \
  "v_add_u32 v194, 419456, v194\n\t" \
  "v_mfma_f32_16x16x32_bf16 v[48:51], v[104:107], v[112:115], v[48:51]\n\t" \
  "ds_read_b64_tr_b16 v[148:149], v168 offset:1024\n\t" \
  "v_lshrrev_b32 v195, 16, v194\n\t" \
  "v_mfma_f32_16x16x32_bf16 v[52:55], v[104:107], v[116:119], v[52:55]\n\t" \
  "ds_read_b64_tr_b16 v[150:151], v168 offset:1280\n\t" \
  "v_mad_u32_u24 v222, v195, s64, v237\n\t" \
  "v_mfma_f32_16x16x32_bf16 v[56:59], v[104:107], v[120:123], v[56:59]\n\t" \
  "ds_read_b64_tr_b16 v[156:157], v169 offset:1024\n\t" \
  "v_add_u32 v194, 419456, v194\n\t" \
  "v_mfma_f32_16x16x32_bf16 v[60:63], v[104:107], v[124:127], v[60:63]\n\t" \
  "ds_read_b64_tr_b16 v[158:159], v169 offset:1280\n\t" \
  "v_lshrrev_b32 v195, 16, v194\n\t" \
  "v_mfma_f32_16x16x32_bf16 v[64:67], v[144:147], v[112:115], v[64:67]\n\t" \
  "v_mad_u32_u24 v223, v195, s64, v237\n\t" \
  "v_add_u32 v194, 419456, v194\n\t" \
  "v_mfma_f32_16x16x32_bf16 v[68:71], v[152:155], v[112:115], v[68:71]\n\t" \
  "v_lshrrev_b32 v195, 16, v194\n\t" \
  "s_waitcnt lgkmcnt(4)\n\t" \
  "v_smfmac_f32_16x16x64_bf16 v[72:75], v[176:179], v[80:87], v174\n\t" \
  "v_mad_u32_u24 v224, v195, s64, v237\n\t" \
  "v_mfma_f32_16x16x32_bf16 v[0:3], v[84:87], v[128:131], v[0:3]\n\t" \
  "v_add_u32 v224, 4096, v224\n\t" \
  "s_mul_i32 s48, s34, 10240\n\t" \
  "s_add_u32 s48, s48, 122880\n\t" \
  "s_and_b32 s64, s62, 7\n\t" \
  "s_mul_i32 s64, s64, 160\n\t" \
  "s_mul_i32 s63, s34, 573440\n\t" \
  "s_add_u32 s63, s63, s64\n\t" \
  "s_add_u32 s36, s22, s63\n\t" \
  "s_addc_u32 s37, s23, 0\n\t" \
  "v_mfma_f32_16x16x32_bf16 v[4:7], v[84:87], v[132:135], v[4:7]\n\t" \
  "v_add_u32 v160, s55, v160\n\t" \
  "v_add_u32 v161, s55, v161\n\t" \
  "v_mfma_f32_16x16x32_bf16 v[8:11], v[84:87], v[136:139], v[8:11]\n\t" \
  "v_add_u32 v162, s55, v162\n\t" \
  "v_add_u32 v163, s55, v163\n\t" \
  "v_add_u32 v168, s55, v168\n\t" \
  "v_add_u32 v169, s55, v169\n\t" \
  "s_setprio 1\n\t" \
  "s_waitcnt vmcnt(4) lgkmcnt(0)\n\t" \
  "s_barrier\n\t" \
  "v_smfmac_f32_16x16x64_bf16 v[76:79], v[176:179], v[144:151], v174\n\t" \
  "ds_read_b128 v[112:115], v164 offset:0\n\t" \
  "v_mfma_f32_16x16x32_bf16 v[12:15], v[84:87], v[140:143], v[12:15]\n\t" \
  "ds_read_b64_tr_b16 v[80:81], v160 offset:0\n\t" \
  "v_add_u32 v166, s55, v166\n\t" \
  "v_mfma_f32_16x16x32_bf16 v[16:19], v[92:95], v[128:131], v[16:19]\n\t" \
  "ds_read_b64_tr_b16 v[82:83], v160 offset:256\n\t" \
  "v_add_u32 v167, s55, v167\n\t" \
  "v_mfma_f32_16x16x32_bf16 v[20:23], v[92:95], v[132:135], v[20:23]\n\t" \
  "ds_read_b128 v[116:119], v165 offset:0\n\t" \
  "s_add_u32 m0, s48, 0\n\t" \
  "v_mfma_f32_16x16x32_bf16 v[24:27], v[92:95], v[136:139], v[24:27]\n\t" \
  "ds_read_b128 v[120:123], v164 offset:4096\n\t" \
  "global_load_lds_dwordx4 v220, s[36:37] offset:0\n\t" \
  "v_mfma_f32_16x16x32_bf16 v[28:31], v[92:95], v[140:143], v[28:31]\n\t" \
  "ds_read_b128 v[124:127], v165 offset:4096\n\t" \
  "global_load_lds_dwordx4 v221, s[36:37] offset:1024\n\t" \
  "v_mfma_f32_16x16x32_bf16 v[32:35], v[100:103], v[128:131], v[32:35]\n\t" \
  "ds_read_b64_tr_b16 v[88:89], v161 offset:0\n\t" \
  "global_load_lds_dwordx4 v222, s[36:37] offset:2048\n\t" \
  "v_mfma_f32_16x16x32_bf16 v[36:39], v[100:103], v[132:135], v[36:39]\n\t" \
  "ds_read_b64_tr_b16 v[90:91], v161 offset:256\n\t" \
  "global_load_lds_dwordx4 v223, s[36:37] offset:3072\n\t" \
  "v_mfma_f32_16x16x32_bf16 v[40:43], v[100:103], v[136:139], v[40:43]\n\t" \
  "ds_read_b64_tr_b16 v[96:97], v162 offset:0\n\t" \
  "s_add_u32 m0, m0, 4096\n\t" \
  "v_mfma_f32_16x16x32_bf16 v[44:47], v[100:103], v[140:143], v[44:47]\n\t" \
  "ds_read_b64_tr_b16 v[98:99], v162 offset:256\n\t" \
  "global_load_lds_dwordx4 v224, s[36:37]\n\t" \
  "v_mfma_f32_16x16x32_bf16 v[48:51], v[108:111], v[128:131], v[48:51]\n\t" \
  "ds_read_b64_tr_b16 v[104:105], v163 offset:0\n\t" \
  "s_add_u32 s36, s36, 40960\n\t" \
  "s_addc_u32 s37, s37, 0\n\t" \
  "v_mfma_f32_16x16x32_bf16 v[52:55], v[108:111], v[132:135], v[52:55]\n\t" \
  "ds_read_b64_tr_b16 v[106:107], v163 offset:256\n\t" \
  "s_add_u32 m0, s48, 5120\n\t" \
  "v_mfma_f32_16x16x32_bf16 v[56:59], v[108:111], v[136:139], v[56:59]\n\t" \
  "ds_read_b64_tr_b16 v[144:145], v168 offset:0\n\t" \
  "global_load_lds_dwordx4 v220, s[36:37] offset:0\n\t" \
  "v_mfma_f32_16x16x32_bf16 v[60:63], v[108:111], v[140:143], v[60:63]\n\t" \
  "ds_read_b64_tr_b16 v[146:147], v168 offset:256\n\t" \
  "global_load_lds_dwordx4 v221, s[36:37] offset:1024\n\t" \
  "v_mfma_f32_16x16x32_bf16 v[64:67], v[148:151], v[128:131], v[64:67]\n\t" \
  "ds_read_b64_tr_b16 v[152:153], v169 offset:0\n\t" \
  "global_load_lds_dwordx4 v222, s[36:37] offset:2048\n\t" \
  "v_mfma_f32_16x16x32_bf16 v[68:71], v[156:159], v[128:131], v[68:71]\n\t" \
  "ds_read_b64_tr_b16 v[154:155], v169 offset:256\n\t" \
  "global_load_lds_dwordx4 v223, s[36:37] offset:3072\n\t" \
  "s_add_u32 m0, m0, 4096\n\t" \
  "s_nop 0\n\t" \
  "global_load_lds_dwordx4 v224, s[36:37]\n\t" \
  "s_add_u32 s36, s36, 40960\n\t" \
  "s_addc_u32 s37, s37, 0\n\t" \
  "s_waitcnt lgkmcnt(4)\n\t" \
  "v_mfma_f32_16x16x32_bf16 v[0:3], v[80:83], v[112:115], v[0:3]\n\t" \
  "ds_read_b128 v[128:131], v166 offset:0\n\t" \
  "s_setprio 0\n\t" \
  "v_mfma_f32_16x16x32_bf16 v[4:7], v[80:83], v[116:119], v[4:7]\n\t" \
  "ds_read_b64_tr_b16 v[84:85], v160 offset:8192\n\t" \
  "v_lshl_add_u32 v175, s32, 6, v229\n\t" \
  "v_mfma_f32_16x16x32_bf16 v[8:11], v[80:83], v[120:123], v[8:11]\n\t" \
  "ds_read_b64_tr_b16 v[86:87], v160 offset:8448\n\t" \
  "v_xor_b32 v232, 64, v175\n\t" \
  "v_mfma_f32_16x16x32_bf16 v[12:15], v[80:83], v[124:127], v[12:15]\n\t" \
  "ds_read_b128 v[132:135], v167 offset:0\n\t" \
  "s_waitcnt lgkmcnt(4)\n\t" \
  "v_mfma_f32_16x16x32_bf16 v[16:19], v[88:91], v[112:115], v[16:19]\n\t" \
  "ds_read_b128 v[136:139], v166 offset:4096\n\t" \
  "v_xor_b32 v172, 128, v175\n\t" \
  "v_mfma_f32_16x16x32_bf16 v[20:23], v[88:91], v[116:119], v[20:23]\n\t" \
  "ds_read_b128 v[140:143], v167 offset:4096\n\t" \
  "v_xor_b32 v173, 192, v175\n\t" \
  "v_mfma_f32_16x16x32_bf16 v[24:27], v[88:91], v[120:123], v[24:27]\n\t" \
  "ds_read_b64_tr_b16 v[92:93], v161 offset:8192\n\t" \
  "v_add_u32 v184, 64, v229\n\t" \
  "v_mfma_f32_16x16x32_bf16 v[28:31], v[88:91], v[124:127], v[28:31]\n\t" \
  "ds_read_b64_tr_b16 v[94:95], v161 offset:8448\n\t" \
  "v_xor_b32 v185, 64, v184\n\t" \
  "v_mfma_f32_16x16x32_bf16 v[32:35], v[96:99], v[112:115], v[32:35]\n\t" \
  "ds_read_b64_tr_b16 v[100:101], v162 offset:8192\n\t" \
  "s_mul_i32 s64, s34, 5120\n\t" \
  "v_mfma_f32_16x16x32_bf16 v[36:39], v[96:99], v[116:119], v[36:39]\n\t" \
  "ds_read_b64_tr_b16 v[102:103], v162 offset:8448\n\t" \
  "s_add_u32 s64, s64, 1024\n\t" \
  "v_mfma_f32_16x16x32_bf16 v[40:43], v[96:99], v[120:123], v[40:43]\n\t" \
  "ds_read_b64_tr_b16 v[108:109], v163 offset:8192\n\t" \
  "s_waitcnt vmcnt(10)\n\t" \
  "v_mfma_f32_16x16x32_bf16 v[44:47], v[96:99], v[124:127], v[44:47]\n\t" \
  "ds_read_b64_tr_b16 v[110:111], v163 offset:8448\n\t" \
  "v_xor_b32 v188, s28, v241\n\t" \
  "v_mfma_f32_16x16x32_bf16 v[48:51], v[104:107], v[112:115], v[48:51]\n\t" \
  "ds_read_b64_tr_b16 v[148:149], v168 offset:1024\n\t" \
  "v_xor_b32 v189, s28, v243\n\t" \
  "v_mfma_f32_16x16x32_bf16 v[52:55], v[104:107], v[116:119], v[52:55]\n\t" \
  "ds_read_b64_tr_b16 v[150:151], v168 offset:1280\n\t" \
  "v_bitop3_b32 v188, v188, s28, v245 bitop3:0xf6\n\t" \
  "v_mfma_f32_16x16x32_bf16 v[56:59], v[104:107], v[120:123], v[56:59]\n\t" \
  "ds_read_b64_tr_b16 v[156:157], v169 offset:1024\n\t" \
  "v_bitop3_b32 v189, v189, s28, v247 bitop3:0xf6\n\t" \
  "v_mfma_f32_16x16x32_bf16 v[60:63], v[104:107], v[124:127], v[60:63]\n\t" \
  "ds_read_b64_tr_b16 v[158:159], v169 offset:1280\n\t" \
  "v_bitop3_b32 v188, v188, s28, v249 bitop3:0xf6\n\t" \
  "v_mfma_f32_16x16x32_bf16 v[64:67], v[144:147], v[112:115], v[64:67]\n\t" \
  "v_bitop3_b32 v189, v189, s28, v251 bitop3:0xf6\n\t" \
  "v_bitop3_b32 v188, v188, s28, v253 bitop3:0xf6\n\t" \
  "v_mfma_f32_16x16x32_bf16 v[68:71], v[152:155], v[112:115], v[68:71]\n\t" \
  "v_bitop3_b32 v189, v189, s28, v255 bitop3:0xf6\n\t" \
  "s_waitcnt lgkmcnt(4)\n\t" \
  "v_smfmac_f32_16x16x64_bf16 v[72:75], v[176:179], v[80:87], v174\n\t" \
  "v_or_b32 v188, v188, v189\n\t" \
  "v_mfma_f32_16x16x32_bf16 v[0:3], v[84:87], v[128:131], v[0:3]\n\t" \
  "v_cmp_ne_u32 vcc, 0, v188\n\t" \
  "v_add_u32 v230, s64, v237\n\t" \
  "v_mfma_f32_16x16x32_bf16 v[4:7], v[84:87], v[132:135], v[4:7]\n\t" \
  "s_cbranch_vccnz .Lstga%=\n\t" \
  ".Lstgb%=:\n\t" \
  "v_mfma_f32_16x16x32_bf16 v[8:11], v[84:87], v[136:139], v[8:11]\n\t" \
  "v_lshl_add_u32 v187, s32, 6, v192\n\t" \
  "s_setprio 1\n\t" \
  "s_waitcnt lgkmcnt(0)\n\t" \
  "s_barrier\n\t" \
  "v_smfmac_f32_16x16x64_bf16 v[76:79], v[176:179], v[144:151], v174\n\t" \
  "v_mul_f32 v242, v242, v242\n\t" \
  "v_mfma_f32_16x16x32_bf16 v[12:15], v[84:87], v[140:143], v[12:15]\n\t" \
  "v_mul_f32 v246, v246, v246\n\t" \
  "v_mul_f32 v250, v250, v250\n\t" \
  "v_mfma_f32_16x16x32_bf16 v[16:19], v[92:95], v[128:131], v[16:19]\n\t" \
  "v_mul_f32 v254, v254, v254\n\t" \
  "v_mfma_f32_16x16x32_bf16 v[20:23], v[92:95], v[132:135], v[20:23]\n\t" \
  "v_add_f32 v72, v72, v73\n\t" \
  "v_add_f32 v76, v76, v77\n\t" \
  "v_mfma_f32_16x16x32_bf16 v[24:27], v[92:95], v[136:139], v[24:27]\n\t" \
  "v_mov_b32 v73, 0\n\t" \
  "s_mov_b64 exec, 0xffff\n\t" \
  "ds_write_b32 v187, v72 offset:0\n\t" \
  "ds_write_b32 v192, v76 offset:576\n\t" \
  "ds_write_b32 v192, v73 offset:768\n\t" \
  "s_mov_b64 exec, -1\n\t" \
  "v_mfma_f32_16x16x32_bf16 v[28:31], v[92:95], v[140:143], v[28:31]\n\t" \
  "s_xor_b32 s64, s59, 0\n\t" \
  "s_mul_i32 s64, s64, 55296\n\t" \
  "v_mfma_f32_16x16x32_bf16 v[32:35], v[100:103], v[128:131], v[32:35]\n\t" \
  "v_add_u32 v214, s64, v237\n\t" \
  "s_xor_b32 s64, s59, 1\n\t" \
  "v_mfma_f32_16x16x32_bf16 v[36:39], v[100:103], v[132:135], v[36:39]\n\t" \
  "s_mul_i32 s64, s64, 55296\n\t" \
  "v_add_u32 v215, s64, v237\n\t" \
  "v_mfma_f32_16x16x32_bf16 v[40:43], v[100:103], v[136:139], v[40:43]\n\t" \
  "s_xor_b32 s64, s59, 2\n\t" \
  "s_mul_i32 s64, s64, 55296\n\t" \
  "v_mfma_f32_16x16x32_bf16 v[44:47], v[100:103], v[140:143], v[44:47]\n\t" \
  "v_add_u32 v216, s64, v237\n\t" \
  "s_xor_b32 s64, s59, 3\n\t" \
  "v_mfma_f32_16x16x32_bf16 v[48:51], v[108:111], v[128:131], v[48:51]\n\t" \
  "s_mul_i32 s64, s64, 55296\n\t" \
  "v_add_u32 v217, s64, v237\n\t" \
  "v_mfma_f32_16x16x32_bf16 v[52:55], v[108:111], v[132:135], v[52:55]\n\t" \
  "v_mov_b32 v218, v214\n\t" \
  "s_mov_b32 exec_lo, 0\n\t" \
  "v_subrev_u32 v218, 512, v215\n\t" \
  "s_mov_b32 exec_lo, -1\n\t" \
  "v_mfma_f32_16x16x32_bf16 v[56:59], v[108:111], v[136:139], v[56:59]\n\t" \
  "v_mfma_f32_16x16x32_bf16 v[60:63], v[108:111], v[140:143], v[60:63]\n\t" \
  "v_mfma_f32_16x16x32_bf16 v[64:67], v[148:151], v[128:131], v[64:67]\n\t" \
  "v_mfma_f32_16x16x32_bf16 v[68:71], v[156:159], v[128:131], v[68:71]\n\t" \
  "s_setprio 0\n\t" \
  "s_waitcnt lgkmcnt(0)\n\t" \
  "s_barrier\n\t" \
  "ds_read_b128 v[128:131], v175 offset:0\n\t" \
  "ds_read_b128 v[132:135], v232 offset:0\n\t" \
  "ds_read_b128 v[136:139], v172 offset:0\n\t" \
  "ds_read_b128 v[140:143], v173 offset:0\n\t" \
  "ds_read_b128 v[144:147], v184 offset:512\n\t" \
  "ds_read_b128 v[148:151], v185 offset:512\n\t" \
  "s_mul_i32 s63, s71, 221184\n\t" \
  "s_add_u32 s63, s63, 3440640\n\t" \
  "s_lshl_b32 s64, s58, 11\n\t" \
  "s_add_u32 s64, s64, s63\n\t" \
  "s_add_u32 s10, s24, s64\n\t" \
  "s_addc_u32 s11, s25, 0\n\t" \
  "s_lshr_b32 s64, s58, 1\n\t" \
  "s_add_u32 s64, s64, 96\n\t" \
  "s_lshl_b32 s64, s64, 9\n\t" \
  "s_add_u32 s63, s63, s64\n\t" \
  "s_add_u32 s12, s24, s63\n\t" \
  "s_addc_u32 s13, s25, 0\n\t" \
  "s_setprio 2\n\t" \
  "s_waitcnt lgkmcnt(5)\n\t" \
  "v_pk_fma_f32 v[0:1], v[240:241], v[128:129], v[0:1] op_sel_hi:[0,1,1] neg_lo:[1,0,0] neg_hi:[1,0,0]\n\t" \
  "v_pk_fma_f32 v[2:3], v[240:241], v[130:131], v[2:3] op_sel_hi:[0,1,1] neg_lo:[1,0,0] neg_hi:[1,0,0]\n\t" \
  "v_pk_fma_f32 v[4:5], v[244:245], v[128:129], v[4:5] op_sel_hi:[0,1,1] neg_lo:[1,0,0] neg_hi:[1,0,0]\n\t" \
  "v_pk_fma_f32 v[6:7], v[244:245], v[130:131], v[6:7] op_sel_hi:[0,1,1] neg_lo:[1,0,0] neg_hi:[1,0,0]\n\t" \
  "v_pk_fma_f32 v[8:9], v[248:249], v[128:129], v[8:9] op_sel_hi:[0,1,1] neg_lo:[1,0,0] neg_hi:[1,0,0]\n\t" \
  "v_pk_fma_f32 v[10:11], v[248:249], v[130:131], v[10:11] op_sel_hi:[0,1,1] neg_lo:[1,0,0] neg_hi:[1,0,0]\n\t" \
  "v_pk_fma_f32 v[12:13], v[252:253], v[128:129], v[12:13] op_sel_hi:[0,1,1] neg_lo:[1,0,0] neg_hi:[1,0,0]\n\t" \
  "v_pk_fma_f32 v[14:15], v[252:253], v[130:131], v[14:15] op_sel_hi:[0,1,1] neg_lo:[1,0,0] neg_hi:[1,0,0]\n\t" \
  "s_waitcnt lgkmcnt(4)\n\t" \
  "v_pk_fma_f32 v[16:17], v[240:241], v[132:133], v[16:17] op_sel_hi:[0,1,1] neg_lo:[1,0,0] neg_hi:[1,0,0]\n\t" \
  "v_pk_fma_f32 v[18:19], v[240:241], v[134:135], v[18:19] op_sel_hi:[0,1,1] neg_lo:[1,0,0] neg_hi:[1,0,0]\n\t" \
  "v_pk_fma_f32 v[20:21], v[244:245], v[132:133], v[20:21] op_sel_hi:[0,1,1] neg_lo:[1,0,0] neg_hi:[1,0,0]\n\t" \
  "v_pk_fma_f32 v[22:23], v[244:245], v[134:135], v[22:23] op_sel_hi:[0,1,1] neg_lo:[1,0,0] neg_hi:[1,0,0]\n\t" \
  "v_pk_fma_f32 v[24:25], v[248:249], v[132:133], v[24:25] op_sel_hi:[0,1,1] neg_lo:[1,0,0] neg_hi:[1,0,0]\n\t" \
  "v_pk_fma_f32 v[26:27], v[248:249], v[134:135], v[26:27] op_sel_hi:[0,1,1] neg_lo:[1,0,0] neg_hi:[1,0,0]\n\t" \
  "v_pk_fma_f32 v[28:29], v[252:253], v[132:133], v[28:29] op_sel_hi:[0,1,1] neg_lo:[1,0,0] neg_hi:[1,0,0]\n\t" \
  "v_pk_fma_f32 v[30:31], v[252:253], v[134:135], v[30:31] op_sel_hi:[0,1,1] neg_lo:[1,0,0] neg_hi:[1,0,0]\n\t" \
  "s_waitcnt lgkmcnt(3)\n\t" \
  "v_pk_fma_f32 v[32:33], v[240:241], v[136:137], v[32:33] op_sel_hi:[0,1,1] neg_lo:[1,0,0] neg_hi:[1,0,0]\n\t" \
  "v_pk_fma_f32 v[34:35], v[240:241], v[138:139], v[34:35] op_sel_hi:[0,1,1] neg_lo:[1,0,0] neg_hi:[1,0,0]\n\t" \
  "v_pk_fma_f32 v[36:37], v[244:245], v[136:137], v[36:37] op_sel_hi:[0,1,1] neg_lo:[1,0,0] neg_hi:[1,0,0]\n\t" \
  "v_pk_fma_f32 v[38:39], v[244:245], v[138:139], v[38:39] op_sel_hi:[0,1,1] neg_lo:[1,0,0] neg_hi:[1,0,0]\n\t" \
  "v_pk_fma_f32 v[40:41], v[248:249], v[136:137], v[40:41] op_sel_hi:[0,1,1] neg_lo:[1,0,0] neg_hi:[1,0,0]\n\t" \
  "v_pk_fma_f32 v[42:43], v[248:249], v[138:139], v[42:43] op_sel_hi:[0,1,1] neg_lo:[1,0,0] neg_hi:[1,0,0]\n\t" \
  "v_pk_fma_f32 v[44:45], v[252:253], v[136:137], v[44:45] op_sel_hi:[0,1,1] neg_lo:[1,0,0] neg_hi:[1,0,0]\n\t" \
  "v_pk_fma_f32 v[46:47], v[252:253], v[138:139], v[46:47] op_sel_hi:[0,1,1] neg_lo:[1,0,0] neg_hi:[1,0,0]\n\t" \
  "s_waitcnt lgkmcnt(2)\n\t" \
  "v_pk_fma_f32 v[48:49], v[240:241], v[140:141], v[48:49] op_sel_hi:[0,1,1] neg_lo:[1,0,0] neg_hi:[1,0,0]\n\t" \
  "v_pk_fma_f32 v[50:51], v[240:241], v[142:143], v[50:51] op_sel_hi:[0,1,1] neg_lo:[1,0,0] neg_hi:[1,0,0]\n\t" \
  "v_pk_fma_f32 v[52:53], v[244:245], v[140:141], v[52:53] op_sel_hi:[0,1,1] neg_lo:[1,0,0] neg_hi:[1,0,0]\n\t" \
  "v_pk_fma_f32 v[54:55], v[244:245], v[142:143], v[54:55] op_sel_hi:[0,1,1] neg_lo:[1,0,0] neg_hi:[1,0,0]\n\t" \
  "v_pk_fma_f32 v[56:57], v[248:249], v[140:141], v[56:57] op_sel_hi:[0,1,1] neg_lo:[1,0,0] neg_hi:[1,0,0]\n\t" \
  "v_pk_fma_f32 v[58:59], v[248:249], v[142:143], v[58:59] op_sel_hi:[0,1,1] neg_lo:[1,0,0] neg_hi:[1,0,0]\n\t" \
  "v_pk_fma_f32 v[60:61], v[252:253], v[140:141], v[60:61] op_sel_hi:[0,1,1] neg_lo:[1,0,0] neg_hi:[1,0,0]\n\t" \
  "v_pk_fma_f32 v[62:63], v[252:253], v[142:143], v[62:63] op_sel_hi:[0,1,1] neg_lo:[1,0,0] neg_hi:[1,0,0]\n\t" \
  "s_waitcnt lgkmcnt(0)\n\t" \
  "v_pk_fma_f32 v[64:65], v[240:241], v[144:145], v[64:65] op_sel_hi:[0,1,1] neg_lo:[1,0,0] neg_hi:[1,0,0]\n\t" \
  "v_pk_fma_f32 v[66:67], v[240:241], v[146:147], v[66:67] op_sel_hi:[0,1,1] neg_lo:[1,0,0] neg_hi:[1,0,0]\n\t" \
  "v_pk_fma_f32 v[68:69], v[240:241], v[148:149], v[68:69] op_sel_hi:[0,1,1] neg_lo:[1,0,0] neg_hi:[1,0,0]\n\t" \
  "v_pk_fma_f32 v[70:71], v[240:241], v[150:151], v[70:71] op_sel_hi:[0,1,1] neg_lo:[1,0,0] neg_hi:[1,0,0]\n\t" \
  "s_bitcmp1_b32 s32, 1\n\t" \
  "s_cbranch_scc1 .Lr1g%=\n\t" \
  "v_max_f32 v0, 0, v0\n\t" \
  "v_max_f32 v1, 0, v1\n\t" \
  "v_max_f32 v2, 0, v2\n\t" \
  "v_max_f32 v3, 0, v3\n\t" \
  "v_max_f32 v4, 0, v4\n\t" \
  "v_max_f32 v5, 0, v5\n\t" \
  "v_max_f32 v6, 0, v6\n\t" \
  "v_max_f32 v7, 0, v7\n\t" \
  "v_max_f32 v8, 0, v8\n\t" \
  "v_max_f32 v9, 0, v9\n\t" \
  "v_max_f32 v10, 0, v10\n\t" \
  "v_max_f32 v11, 0, v11\n\t" \
  "v_max_f32 v12, 0, v12\n\t" \
  "v_max_f32 v13, 0, v13\n\t" \
  "v_max_f32 v14, 0, v14\n\t" \
  "v_max_f32 v15, 0, v15\n\t" \
  "v_max_f32 v16, 0, v16\n\t" \
  "v_max_f32 v17, 0, v17\n\t" \
  "v_max_f32 v18, 0, v18\n\t" \
  "v_max_f32 v19, 0, v19\n\t" \
  "v_max_f32 v20, 0, v20\n\t" \
  "v_max_f32 v21, 0, v21\n\t" \
  "v_max_f32 v22, 0, v22\n\t" \
  "v_max_f32 v23, 0, v23\n\t" \
  "v_max_f32 v24, 0, v24\n\t" \
  "v_max_f32 v25, 0, v25\n\t" \
  "v_max_f32 v26, 0, v26\n\t" \
  "v_max_f32 v27, 0, v27\n\t" \
  "v_max_f32 v28, 0, v28\n\t" \
  "v_max_f32 v29, 0, v29\n\t" \
  "v_max_f32 v30, 0, v30\n\t" \
  "v_max_f32 v31, 0, v31\n\t" \
  "s_branch .Lr2g%=\n\t" \
  ".Lr1g%=:\n\t" \
  "v_max_f32 v32, 0, v32\n\t" \
  "v_max_f32 v33, 0, v33\n\t" \
  "v_max_f32 v34, 0, v34\n\t" \
  "v_max_f32 v35, 0, v35\n\t" \
  "v_max_f32 v36, 0, v36\n\t" \
  "v_max_f32 v37, 0, v37\n\t" \
  "v_max_f32 v38, 0, v38\n\t" \
  "v_max_f32 v39, 0, v39\n\t" \
  "v_max_f32 v40, 0, v40\n\t" \
  "v_max_f32 v41, 0, v41\n\t" \
  "v_max_f32 v42, 0, v42\n\t" \
  "v_max_f32 v43, 0, v43\n\t" \
  "v_max_f32 v44, 0, v44\n\t" \
  "v_max_f32 v45, 0, v45\n\t" \
  "v_max_f32 v46, 0, v46\n\t" \
  "v_max_f32 v47, 0, v47\n\t" \
  "v_max_f32 v48, 0, v48\n\t" \
  "v_max_f32 v49, 0, v49\n\t" \
  "v_max_f32 v50, 0, v50\n\t" \
  "v_max_f32 v51, 0, v51\n\t" \
  "v_max_f32 v52, 0, v52\n\t" \
  "v_max_f32 v53, 0, v53\n\t" \
  "v_max_f32 v54, 0, v54\n\t" \
  "v_max_f32 v55, 0, v55\n\t" \
  "v_max_f32 v56, 0, v56\n\t" \
  "v_max_f32 v57, 0, v57\n\t" \
  "v_max_f32 v58, 0, v58\n\t" \
  "v_max_f32 v59, 0, v59\n\t" \
  "v_max_f32 v60, 0, v60\n\t" \
  "v_max_f32 v61, 0, v61\n\t" \
  "v_max_f32 v62, 0, v62\n\t" \
  "v_max_f32 v63, 0, v63\n\t" \
  ".Lr2g%=:\n\t" \
  "v_pk_mul_f32 v[0:1], v[0:1], v[32:33]\n\t" \
  "v_pk_mul_f32 v[2:3], v[2:3], v[34:35]\n\t" \
  "v_pk_mul_f32 v[4:5], v[4:5], v[36:37]\n\t" \
  "v_pk_mul_f32 v[6:7], v[6:7], v[38:39]\n\t" \
  "v_pk_mul_f32 v[8:9], v[8:9], v[40:41]\n\t" \
  "v_pk_mul_f32 v[10:11], v[10:11], v[42:43]\n\t" \
  "v_pk_mul_f32 v[12:13], v[12:13], v[44:45]\n\t" \
  "v_pk_mul_f32 v[14:15], v[14:15], v[46:47]\n\t" \
  "v_pk_mul_f32 v[16:17], v[16:17], v[48:49]\n\t" \
  "v_pk_mul_f32 v[18:19], v[18:19], v[50:51]\n\t" \
  "v_pk_mul_f32 v[20:21], v[20:21], v[52:53]\n\t" \
  "v_pk_mul_f32 v[22:23], v[22:23], v[54:55]\n\t" \
  "v_pk_mul_f32 v[24:25], v[24:25], v[56:57]\n\t" \
  "v_pk_mul_f32 v[26:27], v[26:27], v[58:59]\n\t" \
  "v_pk_mul_f32 v[28:29], v[28:29], v[60:61]\n\t" \
  "v_pk_mul_f32 v[30:31], v[30:31], v[62:63]\n\t" \
  "v_pk_mul_f32 v[0:1], v[242:243], v[0:1] op_sel_hi:[0,1]\n\t" \
  "v_pk_mul_f32 v[2:3], v[242:243], v[2:3] op_sel_hi:[0,1]\n\t" \
  "v_pk_mul_f32 v[4:5], v[246:247], v[4:5] op_sel_hi:[0,1]\n\t" \
  "v_pk_mul_f32 v[6:7], v[246:247], v[6:7] op_sel_hi:[0,1]\n\t" \
  "v_pk_mul_f32 v[8:9], v[250:251], v[8:9] op_sel_hi:[0,1]\n\t" \
  "v_pk_mul_f32 v[10:11], v[250:251], v[10:11] op_sel_hi:[0,1]\n\t" \
  "v_pk_mul_f32 v[12:13], v[254:255], v[12:13] op_sel_hi:[0,1]\n\t" \
  "v_pk_mul_f32 v[14:15], v[254:255], v[14:15] op_sel_hi:[0,1]\n\t" \
  "v_pk_mul_f32 v[16:17], v[242:243], v[16:17] op_sel_hi:[0,1]\n\t" \
  "v_pk_mul_f32 v[18:19], v[242:243], v[18:19] op_sel_hi:[0,1]\n\t" \
  "v_pk_mul_f32 v[20:21], v[246:247], v[20:21] op_sel_hi:[0,1]\n\t" \
  "v_pk_mul_f32 v[22:23], v[246:247], v[22:23] op_sel_hi:[0,1]\n\t" \
  "v_pk_mul_f32 v[24:25], v[250:251], v[24:25] op_sel_hi:[0,1]\n\t" \
  "v_pk_mul_f32 v[26:27], v[250:251], v[26:27] op_sel_hi:[0,1]\n\t" \
  "v_pk_mul_f32 v[28:29], v[254:255], v[28:29] op_sel_hi:[0,1]\n\t" \
  "v_pk_mul_f32 v[30:31], v[254:255], v[30:31] op_sel_hi:[0,1]\n\t" \
  "s_bitcmp1_b32 s32, 0\n\t" \
  "s_cbranch_scc1 .Lr5g%=\n\t" \
  "v_cvt_pk_bf16_f32 v96, v0, v1\n\t" \
  "v_cvt_pk_bf16_f32 v97, v2, v3\n\t" \
  "v_cvt_pk_bf16_f32 v98, v16, v17\n\t" \
  "v_cvt_pk_bf16_f32 v99, v18, v19\n\t" \
  "v_cvt_pk_bf16_f32 v100, v4, v5\n\t" \
  "v_cvt_pk_bf16_f32 v101, v6, v7\n\t" \
  "v_cvt_pk_bf16_f32 v102, v20, v21\n\t" \
  "v_cvt_pk_bf16_f32 v103, v22, v23\n\t" \
  "v_cvt_pk_bf16_f32 v104, v8, v9\n\t" \
  "v_cvt_pk_bf16_f32 v105, v10, v11\n\t" \
  "v_cvt_pk_bf16_f32 v106, v24, v25\n\t" \
  "v_cvt_pk_bf16_f32 v107, v26, v27\n\t" \
  "v_cvt_pk_bf16_f32 v108, v12, v13\n\t" \
  "v_cvt_pk_bf16_f32 v109, v14, v15\n\t" \
  "v_cvt_pk_bf16_f32 v110, v28, v29\n\t" \
  "v_cvt_pk_bf16_f32 v111, v30, v31\n\t" \
  "s_branch .Lr6g%=\n\t" \
  ".Lr5g%=:\n\t" \
  "v_cvt_pk_bf16_f32 v98, v0, v1\n\t" \
  "v_cvt_pk_bf16_f32 v99, v2, v3\n\t" \
  "v_cvt_pk_bf16_f32 v96, v16, v17\n\t" \
  "v_cvt_pk_bf16_f32 v97, v18, v19\n\t" \
  "v_cvt_pk_bf16_f32 v102, v4, v5\n\t" \
  "v_cvt_pk_bf16_f32 v103, v6, v7\n\t" \
  "v_cvt_pk_bf16_f32 v100, v20, v21\n\t" \
  "v_cvt_pk_bf16_f32 v101, v22, v23\n\t" \
  "v_cvt_pk_bf16_f32 v106, v8, v9\n\t" \
  "v_cvt_pk_bf16_f32 v107, v10, v11\n\t" \
  "v_cvt_pk_bf16_f32 v104, v24, v25\n\t" \
  "v_cvt_pk_bf16_f32 v105, v26, v27\n\t" \
  "v_cvt_pk_bf16_f32 v110, v12, v13\n\t" \
  "v_cvt_pk_bf16_f32 v111, v14, v15\n\t" \
  "v_cvt_pk_bf16_f32 v108, v28, v29\n\t" \
  "v_cvt_pk_bf16_f32 v109, v30, v31\n\t" \
  ".Lr6g%=:\n\t" \
  "v_max_f32 v68, 0, v68\n\t" \
  "v_max_f32 v69, 0, v69\n\t" \
  "v_max_f32 v70, 0, v70\n\t" \
  "v_max_f32 v71, 0, v71\n\t" \
  "v_pk_mul_f32 v[64:65], v[64:65], v[68:69]\n\t" \
  "v_pk_mul_f32 v[66:67], v[66:67], v[70:71]\n\t" \
  "v_pk_mul_f32 v[64:65], v[242:243], v[64:65] op_sel_hi:[0,1]\n\t" \
  "v_pk_mul_f32 v[66:67], v[242:243], v[66:67] op_sel_hi:[0,1]\n\t" \
  "v_cvt_pk_bf16_f32 v114, v64, v65\n\t" \
  "v_cvt_pk_bf16_f32 v115, v66, v67\n\t" \
  ".Lgsetup%=:\n\t" \
  "v_mov_b32 v240, v237\n\t" \
  "v_add_u32 v242, 55296, v237\n\t" \
  "v_add_u32 v244, 110592, v237\n\t" \
  "v_add_u32 v246, 165888, v237\n\t" \
  "v_lshrrev_b32 v194, 2, v228\n\t" \
  "v_mul_u32_u24 v194, 160, v194\n\t" \
  "v_and_b32 v195, 3, v228\n\t" \
  "v_lshl_add_u32 v194, v195, 3, v194\n\t" \
  "v_add_u32 v225, s48, v194\n\t" \
  "v_bfe_u32 v194, v228, 2, 2\n\t" \
  "v_mul_u32_u24 v194, 10240, v194\n\t" \
  "v_and_or_b32 v194, v237, 48, v194\n\t" \
  "v_lshl_add_u32 v227, v229, 2, v194\n\t" \
  "v_add_u32 v236, 40960, v227\n\t" \
  "v_and_b32 v194, 15, v228\n\t" \
  "v_mul_u32_u24 v194, 336, v194\n\t" \
  "s_mul_i32 s63, s34, 21504\n\t" \
  "s_add_u32 s63, s63, 21504\n\t" \
  "v_add3_u32 v226, v194, v229, s63\n\t" \
  "v_mov_b32 v238, 808\n\t" \
  "s_and_b32 s64, s62, 7\n\t" \
  "s_mul_i32 s64, s64, 160\n\t" \
  "s_mul_i32 s63, s34, 204800\n\t" \
  "s_add_u32 s63, s63, s64\n\t" \
  "s_add_u32 s40, s16, s63\n\t" \
  "s_addc_u32 s41, s17, 0\n\t" \
  "s_lshr_b32 s65, s62, 3\n\t" \
  "s_mul_i32 s63, s65, 221184\n\t" \
  "s_mul_i32 s64, s34, 14336\n\t" \
  "s_add_u32 s63, s63, s64\n\t" \
  "s_add_u32 s63, s63, 3440640\n\t" \
  "s_add_u32 s38, s24, s63\n\t" \
  "s_addc_u32 s39, s25, 0\n\t" \
  "s_cmp_eq_u32 s34, 3\n\t" \
  "s_cselect_b32 s49, 6, 7\n\t" \
  "s_cselect_b32 s63, 23, 6\n\t" \
  "s_cselect_b32 s64, 0, s34\n\t" \
  "s_mul_i32 s64, s64, 7\n\t" \
  "s_lshl_b32 s66, s60, 5\n\t" \
  "s_add_u32 s64, s64, s66\n\t" \
  "s_add_u32 s64, s64, 8\n\t" \
  "v_min_u32 v233, s63, v228\n\t" \
  "v_add_lshl_u32 v233, v233, s64, 7\n\t" \
  "v_and_b32 v195, 3, v228\n\t" \
  "v_mul_u32_u24 v194, 10304, v195\n\t" \
  "v_add_u32 v194, 21504, v194\n\t" \
  "v_mul_u32_u24 v195, 2080, v195\n\t" \
  "v_add_u32 v195, 103936, v195\n\t" \
  "v_bfe_u32 v196, v228, 2, 2\n\t" \
  "v_lshrrev_b32 v197, 4, v228\n\t" \
  "v_lshl_or_b32 v196, v197, 3, v196\n\t" \
  "s_lshl_b32 s63, s34, 5\n\t" \
  "v_add_u32 v197, s63, v196\n\t" \
  "v_lshl_add_u32 v241, v197, 4, v194\n\t" \
  "v_mul_u32_u24 v197, 205, v197\n\t" \
  "v_lshrrev_b32 v243, 10, v197\n\t" \
  "v_add_u32 v197, 820, v197\n\t" \
  "v_lshrrev_b32 v245, 10, v197\n\t" \
  "v_lshl_add_u32 v243, v243, 4, v195\n\t" \
  "v_lshl_add_u32 v245, v245, 4, v195\n\t" \
  "s_cmp_eq_u32 s34, 3\n\t" \
  "s_cbranch_scc0 .Lgn3%=\n\t" \
  "v_add_u32 v197, 128, v196\n\t" \
  "v_lshl_add_u32 v247, v197, 4, v194\n\t" \
  "v_mul_u32_u24 v197, 205, v197\n\t" \
  "v_lshrrev_b32 v249, 10, v197\n\t" \
  "v_add_u32 v197, 820, v197\n\t" \
  "v_lshrrev_b32 v251, 10, v197\n\t" \
  "v_lshl_add_u32 v249, v249, 4, v195\n\t" \
  "v_lshl_add_u32 v251, v251, 4, v195\n\t" \
  ".Lgn3%=:\n\t" \
  "s_mul_i32 s63, s62, 10240\n\t" \
  "s_lshl_b32 s64, s34, 10\n\t" \
  "s_add_u32 s63, s63, s64\n\t" \
  "s_add_u32 s63, s63, 10522624\n\t" \
  "s_add_u32 s44, s24, s63\n\t" \
  "s_addc_u32 s45, s25, 0\n\t" \
  "s_lshr_b32 s63, s62, 3\n\t" \
  "s_mul_i32 s63, s63, 81920\n\t" \
  "s_mul_i32 s64, s34, 1280\n\t" \
  "s_add_u32 s63, s63, s64\n\t" \
  "s_add_u32 s63, s63, 10525696\n\t" \
  "s_add_u32 s46, s24, s63\n\t" \
  "s_addc_u32 s47, s25, 0\n\t" \
  "s_mov_b64 s[50:51], -1\n\t" \
  "s_mov_b64 s[52:53], -1\n\t" \
  "s_mov_b32 s43, 0x3c000000\n\t" \
  "s_add_u32 s16, s40, 40960\n\t" \
  "s_addc_u32 s17, s41, 0\n\t" \
  "s_mul_i32 s42, s34, 15360\n\t" \
  "s_cmp_ge_u32 s34, 2\n\t" \
  "s_cselect_b32 s63, 25600, 0\n\t" \
  "s_add_u32 s42, s42, s63\n\t" \
  "s_add_u32 s42, s42, 21504\n\t" \
  "v_mov_b64 v[234:235], 0\n\t" \
  "s_nop 1\n\t" \
  "v_mfma_f32_32x32x8_bf16 v[0:15], v[234:235], v[234:235], 0\n\t" \
  "v_mfma_f32_32x32x8_bf16 v[16:31], v[234:235], v[234:235], 0\n\t" \
  "v_mfma_f32_32x32x8_bf16 v[32:47], v[234:235], v[234:235], 0\n\t" \
  "v_mfma_f32_32x32x8_bf16 v[48:63], v[234:235], v[234:235], 0\n\t" \
  "v_mfma_f32_32x32x8_bf16 v[64:79], v[234:235], v[234:235], 0\n\t" \
  "s_lshl_b32 s63, s34, 2\n\t" \
  "v_mov_b32 v123, s63\n\t" \
  ".Lgh%=:\n\t" \
  "s_sleep 1\n\t" \
  "ds_read_b32 v122, v123 offset:768\n\t" \
  "s_waitcnt lgkmcnt(0)\n\t" \
  "v_cmp_eq_u32 vcc, 0, v122\n\t" \
  "s_nop 1\n\t" \
  "s_cbranch_vccnz .Lgh%=\n\t" \
  "s_cmp_lt_u32 s33, 8\n\t" \
  "s_cbranch_scc1 .Lgq%=\n\t" \
  "global_store_dwordx4 v215, v[96:99], s[10:11] offset:1024 sc1\n\t" \
  "global_store_dwordx4 v214, v[100:103], s[10:11] offset:1024 sc1\n\t" \
  "global_store_dwordx4 v217, v[104:107], s[10:11] offset:1024 sc1\n\t" \
  "global_store_dwordx4 v216, v[108:111], s[10:11] offset:1024 sc1\n\t" \
  ".Lgh2%=:\n\t" \
  "s_sleep 1\n\t" \
  "ds_read_b32 v122, v123 offset:784\n\t" \
  "s_waitcnt lgkmcnt(0)\n\t" \
  "v_cmp_eq_u32 vcc, 0, v122\n\t" \
  "s_nop 1\n\t" \
  "s_cbranch_vccnz .Lgh2%=\n\t" \
  "ds_read_b128 v[128:131], v230 offset:0\n\t" \
  "ds_read_b128 v[132:135], v230 offset:1024\n\t" \
  "ds_read_b128 v[136:139], v230 offset:2048\n\t" \
  "ds_read_b128 v[140:143], v230 offset:3072\n\t" \
  "ds_read_b64 v[112:113], v230 offset:4096\n\t" \
  "s_waitcnt lgkmcnt(4)\n\t" \
  "global_store_dwordx4 v214, v[128:131], s[10:11] sc1\n\t" \
  "s_waitcnt lgkmcnt(3)\n\t" \
  "global_store_dwordx4 v215, v[132:135], s[10:11] sc1\n\t" \
  "s_waitcnt lgkmcnt(2)\n\t" \
  "global_store_dwordx4 v216, v[136:139], s[10:11] sc1\n\t" \
  "s_waitcnt lgkmcnt(1)\n\t" \
  "global_store_dwordx4 v217, v[140:143], s[10:11] sc1\n\t" \
  "s_waitcnt lgkmcnt(0)\n\t" \
  "v_permlane32_swap_b32 v112, v114\n\t" \
  "v_permlane32_swap_b32 v113, v115\n\t" \
  "global_store_dwordx4 v218, v[112:115], s[12:13] sc1\n\t" \
  "v_mov_b64 v[152:153], s[28:29]\n\t" \
  "v_mov_b64 v[154:155], s[28:29]\n\t" \
  "v_mov_b32 v122, 800\n\t" \
  "v_mov_b32 v123, 1\n\t" \
  "s_waitcnt vmcnt(0)\n\t" \
  "s_mov_b64 exec, 1\n\t" \
  "ds_add_rtn_u32 v123, v122, v123\n\t" \
  "s_mov_b64 exec, -1\n\t" \
  "s_waitcnt lgkmcnt(0)\n\t" \
  "v_readfirstlane_b32 s63, v123\n\t" \
  "s_nop 1\n\t" \
  "s_cmp_eq_u32 s63, 3\n\t" \
  "s_cbranch_scc0 .Lgnoh%=\n\t" \
  "global_store_dwordx4 v231, v[152:155], s[24:25] sc1\n\t" \
  ".Lgnoh%=:\n\t" \
  "s_add_u32 m0, s42, 0\n\t" \
  "s_nop 0\n\t" \
  "global_load_lds_dwordx4 v220, s[16:17] offset:0\n\t" \
  "global_load_lds_dwordx4 v221, s[16:17] offset:1024\n\t" \
  "global_load_lds_dwordx4 v222, s[16:17] offset:2048\n\t" \
  "global_load_lds_dwordx4 v223, s[16:17] offset:3072\n\t" \
  "s_add_u32 m0, m0, 4096\n\t" \
  "s_nop 0\n\t" \
  "global_load_lds_dwordx4 v224, s[16:17]\n\t" \
  "s_add_u32 s16, s16, 40960\n\t" \
  "s_addc_u32 s17, s17, 0\n\t" \
  "s_waitcnt vmcnt(0)\n\t" \
  "s_add_u32 m0, s42, 5120\n\t" \
  "s_nop 0\n\t" \
  "global_load_lds_dwordx4 v220, s[16:17] offset:0\n\t" \
  "global_load_lds_dwordx4 v221, s[16:17] offset:1024\n\t" \
  "global_load_lds_dwordx4 v222, s[16:17] offset:2048\n\t" \
  "global_load_lds_dwordx4 v223, s[16:17] offset:3072\n\t" \
  "s_add_u32 m0, m0, 4096\n\t" \
  "s_nop 0\n\t" \
  "global_load_lds_dwordx4 v224, s[16:17]\n\t" \
  "s_add_u32 s16, s16, 40960\n\t" \
  "s_addc_u32 s17, s17, 0\n\t" \
  "s_waitcnt vmcnt(0)\n\t" \
  "s_add_u32 m0, s42, 10240\n\t" \
  "s_nop 0\n\t" \
  "global_load_lds_dwordx4 v220, s[16:17] offset:0\n\t" \
  "global_load_lds_dwordx4 v221, s[16:17] offset:1024\n\t" \
  "global_load_lds_dwordx4 v222, s[16:17] offset:2048\n\t" \
  "global_load_lds_dwordx4 v223, s[16:17] offset:3072\n\t" \
  "s_add_u32 m0, m0, 4096\n\t" \
  "s_nop 0\n\t" \
  "global_load_lds_dwordx4 v224, s[16:17]\n\t" \
  "s_add_u32 s16, s16, 40960\n\t" \
  "s_addc_u32 s17, s17, 0\n\t" \
  "s_waitcnt vmcnt(0)\n\t" \
  "s_sub_u32 s63, s42, s48\n\t" \
  "v_add_u32 v248, s63, v225\n\t" \
  "v_mov_b32 v250, 1\n\t" \
  "s_waitcnt vmcnt(0)\n\t" \
  "ds_read_b64_tr_b16 v[100:101], v248 offset:0\n\t" \
  "ds_read_b64_tr_b16 v[102:103], v248 offset:2560\n\t" \
  "ds_read_b64_tr_b16 v[104:105], v248 offset:32\n\t" \
  "ds_read_b64_tr_b16 v[106:107], v248 offset:2592\n\t" \
  "ds_read_b64_tr_b16 v[108:109], v248 offset:64\n\t" \
  "ds_read_b64_tr_b16 v[110:111], v248 offset:2624\n\t" \
  "ds_read_b64_tr_b16 v[112:113], v248 offset:96\n\t" \
  "ds_read_b64_tr_b16 v[114:115], v248 offset:2656\n\t" \
  "ds_read_b64_tr_b16 v[116:117], v248 offset:128\n\t" \
  "ds_read_b64_tr_b16 v[118:119], v248 offset:2688\n\t" \
  "ds_read_b64_tr_b16 v[120:121], v248 offset:5120\n\t" \
  "ds_read_b64_tr_b16 v[122:123], v248 offset:7680\n\t" \
  "ds_read_b64_tr_b16 v[124:125], v248 offset:5152\n\t" \
  "ds_read_b64_tr_b16 v[126:127], v248 offset:7712\n\t" \
  "ds_read_b64_tr_b16 v[128:129], v248 offset:5184\n\t" \
  "ds_read_b64_tr_b16 v[130:131], v248 offset:7744\n\t" \
  "ds_read_b64_tr_b16 v[132:133], v248 offset:5216\n\t" \
  "ds_read_b64_tr_b16 v[134:135], v248 offset:7776\n\t" \
  "ds_read_b64_tr_b16 v[136:137], v248 offset:5248\n\t" \
  "ds_read_b64_tr_b16 v[138:139], v248 offset:7808\n\t" \
  "ds_read_b64_tr_b16 v[140:141], v248 offset:10240\n\t" \
  "ds_read_b64_tr_b16 v[142:143], v248 offset:12800\n\t" \
  "ds_read_b64_tr_b16 v[144:145], v248 offset:10272\n\t" \
  "ds_read_b64_tr_b16 v[146:147], v248 offset:12832\n\t" \
  "ds_read_b64_tr_b16 v[148:149], v248 offset:10304\n\t" \
  "ds_read_b64_tr_b16 v[150:151], v248 offset:12864\n\t" \
  "ds_read_b64_tr_b16 v[152:153], v248 offset:10336\n\t" \
  "ds_read_b64_tr_b16 v[154:155], v248 offset:12896\n\t" \
  "ds_read_b64_tr_b16 v[156:157], v248 offset:10368\n\t" \
  "ds_read_b64_tr_b16 v[158:159], v248 offset:12928\n\t" \
  "s_mov_b64 exec, 1\n\t" \
  "ds_add_u32 v238, v250 offset:4\n\t" \
  "s_mov_b64 exec, -1\n\t" \
  "s_setprio 0\n\t" \
  "s_mov_b32 s63, 0x400000\n\t" \
  ".Lfga%=:\n\t" \
  "global_load_dwordx2 v[234:235], v233, s[30:31] sc1\n\t" \
  "s_sub_u32 s63, s63, 1\n\t" \
  "s_waitcnt vmcnt(0)\n\t" \
  "v_cmp_ne_u64 vcc, s[28:29], v[234:235]\n\t" \
  "s_nop 1\n\t" \
  "s_cbranch_vccz .Lfgb%=\n\t" \
  "s_sleep 3\n\t" \
  "s_cbranch_scc0 .Lfga%=\n\t" \
  ".Lfgb%=:\n\t" \
  "global_load_dwordx4 v[180:183], v240, s[38:39] sc1\n\t" \
  "global_load_dwordx4 v[184:187], v242, s[38:39] sc1\n\t" \
  "global_load_dwordx4 v[188:191], v244, s[38:39] sc1\n\t" \
  "global_load_dwordx4 v[192:195], v246, s[38:39] sc1\n\t" \
  "s_add_u32 s38, s38, 1024\n\t" \
  "s_addc_u32 s39, s39, 0\n\t" \
  "global_load_dwordx4 v[196:199], v240, s[38:39] sc1\n\t" \
  "global_load_dwordx4 v[200:203], v242, s[38:39] sc1\n\t" \
  "ds_read_b64_tr_b16 v[80:81], v225 offset:0\n\t" \
  "ds_read_b64_tr_b16 v[82:83], v225 offset:2560\n\t" \
  "ds_read_b64_tr_b16 v[84:85], v225 offset:32\n\t" \
  "ds_read_b64_tr_b16 v[86:87], v225 offset:2592\n\t" \
  "ds_read_b64_tr_b16 v[88:89], v225 offset:64\n\t" \
  "ds_read_b64_tr_b16 v[90:91], v225 offset:2624\n\t" \
  "ds_read_b64_tr_b16 v[92:93], v225 offset:96\n\t" \
  "ds_read_b64_tr_b16 v[94:95], v225 offset:2656\n\t" \
  "ds_read_b64_tr_b16 v[96:97], v225 offset:128\n\t" \
  "ds_read_b64_tr_b16 v[98:99], v225 offset:2688\n\t" \
  "s_waitcnt lgkmcnt(0)\n\t" \
  "s_add_u32 m0, s48, 0\n\t" \
  "s_nop 0\n\t" \
  "global_load_lds_dwordx4 v220, s[36:37] offset:0\n\t" \
  "global_load_lds_dwordx4 v221, s[36:37] offset:1024\n\t" \
  "global_load_lds_dwordx4 v222, s[36:37] offset:2048\n\t" \
  "global_load_lds_dwordx4 v223, s[36:37] offset:3072\n\t" \
  "s_add_u32 m0, m0, 4096\n\t" \
  "s_nop 0\n\t" \
  "global_load_lds_dwordx4 v224, s[36:37]\n\t" \
  "s_add_u32 s36, s36, 40960\n\t" \
  "s_addc_u32 s37, s37, 0\n\t" \
  "global_load_dwordx4 v[204:207], v244, s[38:39] sc1\n\t" \
  "global_load_dwordx4 v[208:211], v246, s[38:39] sc1\n\t" \
  "s_add_u32 s38, s38, 1024\n\t" \
  "s_addc_u32 s39, s39, 0\n\t" \
  "s_waitcnt vmcnt(9)\n\t" \
  ".Lffn%=:\n\t" \
  "v_mfma_f32_16x16x32_bf16 v[0:3], v[80:83], v[180:183], v[0:3]\n\t" \
  "ds_read_b64_tr_b16 v[160:161], v225 offset:5120\n\t" \
  "v_mfma_f32_16x16x32_bf16 v[4:7], v[84:87], v[180:183], v[4:7]\n\t" \
  "ds_read_b64_tr_b16 v[162:163], v225 offset:7680\n\t" \
  "v_mfma_f32_16x16x32_bf16 v[8:11], v[88:91], v[180:183], v[8:11]\n\t" \
  "ds_read_b64_tr_b16 v[164:165], v225 offset:5152\n\t" \
  "v_mfma_f32_16x16x32_bf16 v[12:15], v[92:95], v[180:183], v[12:15]\n\t" \
  "ds_read_b64_tr_b16 v[166:167], v225 offset:7712\n\t" \
  "v_mfma_f32_16x16x32_bf16 v[16:19], v[96:99], v[180:183], v[16:19]\n\t" \
  "ds_read_b64_tr_b16 v[168:169], v225 offset:5184\n\t" \
  "global_load_dwordx4 v[180:183], v240, s[38:39] sc1\n\t" \
  "v_mfma_f32_16x16x32_bf16 v[20:23], v[80:83], v[184:187], v[20:23]\n\t" \
  "ds_read_b64_tr_b16 v[170:171], v225 offset:7744\n\t" \
  "v_mfma_f32_16x16x32_bf16 v[24:27], v[84:87], v[184:187], v[24:27]\n\t" \
  "ds_read_b64_tr_b16 v[172:173], v225 offset:5216\n\t" \
  "v_mfma_f32_16x16x32_bf16 v[28:31], v[88:91], v[184:187], v[28:31]\n\t" \
  "ds_read_b64_tr_b16 v[174:175], v225 offset:7776\n\t" \
  "v_mfma_f32_16x16x32_bf16 v[32:35], v[92:95], v[184:187], v[32:35]\n\t" \
  "ds_read_b64_tr_b16 v[176:177], v225 offset:5248\n\t" \
  "v_mfma_f32_16x16x32_bf16 v[36:39], v[96:99], v[184:187], v[36:39]\n\t" \
  "ds_read_b64_tr_b16 v[178:179], v225 offset:7808\n\t" \
  "global_load_dwordx4 v[184:187], v242, s[38:39] sc1\n\t" \
  "s_waitcnt vmcnt(11)\n\t" \
  "v_mfma_f32_16x16x32_bf16 v[40:43], v[80:83], v[188:191], v[40:43]\n\t" \
  "s_waitcnt lgkmcnt(0)\n\t" \
  "s_cmp_eq_u32 s49, 1\n\t" \
  "s_cmov_b64 s[36:37], s[16:17]\n\t" \
  "s_add_u32 m0, s48, 5120\n\t" \
  "v_mfma_f32_16x16x32_bf16 v[44:47], v[84:87], v[188:191], v[44:47]\n\t" \
  "global_load_lds_dwordx4 v220, s[36:37] offset:0\n\t" \
  "global_load_lds_dwordx4 v221, s[36:37] offset:1024\n\t" \
  "v_mfma_f32_16x16x32_bf16 v[48:51], v[88:91], v[188:191], v[48:51]\n\t" \
  "global_load_lds_dwordx4 v222, s[36:37] offset:2048\n\t" \
  "global_load_lds_dwordx4 v223, s[36:37] offset:3072\n\t" \
  "s_add_u32 m0, m0, 4096\n\t" \
  "v_mfma_f32_16x16x32_bf16 v[52:55], v[92:95], v[188:191], v[52:55]\n\t" \
  "global_load_lds_dwordx4 v224, s[36:37]\n\t" \
  "s_add_u32 s36, s36, 40960\n\t" \
  "s_addc_u32 s37, s37, 0\n\t" \
  "v_mfma_f32_16x16x32_bf16 v[56:59], v[96:99], v[188:191], v[56:59]\n\t" \
  "global_load_dwordx4 v[188:191], v244, s[38:39] sc1\n\t" \
  "v_mfma_f32_16x16x32_bf16 v[60:63], v[80:83], v[192:195], v[60:63]\n\t" \
  "v_mfma_f32_16x16x32_bf16 v[64:67], v[84:87], v[192:195], v[64:67]\n\t" \
  "v_mfma_f32_16x16x32_bf16 v[68:71], v[88:91], v[192:195], v[68:71]\n\t" \
  "v_mfma_f32_16x16x32_bf16 v[72:75], v[92:95], v[192:195], v[72:75]\n\t" \
  "v_mfma_f32_16x16x32_bf16 v[76:79], v[96:99], v[192:195], v[76:79]\n\t" \
  "global_load_dwordx4 v[192:195], v246, s[38:39] sc1\n\t" \
  "s_add_u32 s38, s38, 1024\n\t" \
  "s_addc_u32 s39, s39, 0\n\t" \
  "s_waitcnt vmcnt(11)\n\t" \
  "v_mfma_f32_16x16x32_bf16 v[0:3], v[160:163], v[196:199], v[0:3]\n\t" \
  "ds_read_b64_tr_b16 v[80:81], v225 offset:0\n\t" \
  "v_mfma_f32_16x16x32_bf16 v[4:7], v[164:167], v[196:199], v[4:7]\n\t" \
  "ds_read_b64_tr_b16 v[82:83], v225 offset:2560\n\t" \
  "v_mfma_f32_16x16x32_bf16 v[8:11], v[168:171], v[196:199], v[8:11]\n\t" \
  "ds_read_b64_tr_b16 v[84:85], v225 offset:32\n\t" \
  "v_mfma_f32_16x16x32_bf16 v[12:15], v[172:175], v[196:199], v[12:15]\n\t" \
  "ds_read_b64_tr_b16 v[86:87], v225 offset:2592\n\t" \
  "v_mfma_f32_16x16x32_bf16 v[16:19], v[176:179], v[196:199], v[16:19]\n\t" \
  "ds_read_b64_tr_b16 v[88:89], v225 offset:64\n\t" \
  "global_load_dwordx4 v[196:199], v240, s[38:39] sc1\n\t" \
  "v_mfma_f32_16x16x32_bf16 v[20:23], v[160:163], v[200:203], v[20:23]\n\t" \
  "ds_read_b64_tr_b16 v[90:91], v225 offset:2624\n\t" \
  "v_mfma_f32_16x16x32_bf16 v[24:27], v[164:167], v[200:203], v[24:27]\n\t" \
  "ds_read_b64_tr_b16 v[92:93], v225 offset:96\n\t" \
  "v_mfma_f32_16x16x32_bf16 v[28:31], v[168:171], v[200:203], v[28:31]\n\t" \
  "ds_read_b64_tr_b16 v[94:95], v225 offset:2656\n\t" \
  "v_mfma_f32_16x16x32_bf16 v[32:35], v[172:175], v[200:203], v[32:35]\n\t" \
  "ds_read_b64_tr_b16 v[96:97], v225 offset:128\n\t" \
  "v_mfma_f32_16x16x32_bf16 v[36:39], v[176:179], v[200:203], v[36:39]\n\t" \
  "ds_read_b64_tr_b16 v[98:99], v225 offset:2688\n\t" \
  "global_load_dwordx4 v[200:203], v242, s[38:39] sc1\n\t" \
  "s_waitcnt vmcnt(11)\n\t" \
  "v_mfma_f32_16x16x32_bf16 v[40:43], v[160:163], v[204:207], v[40:43]\n\t" \
  "s_waitcnt lgkmcnt(0)\n\t" \
  "s_cmp_le_u32 s49, 2\n\t" \
  "s_cmov_b64 s[36:37], s[40:41]\n\t" \
  "s_add_u32 m0, s48, 0\n\t" \
  "v_mfma_f32_16x16x32_bf16 v[44:47], v[164:167], v[204:207], v[44:47]\n\t" \
  "global_load_lds_dwordx4 v220, s[36:37] offset:0\n\t" \
  "global_load_lds_dwordx4 v221, s[36:37] offset:1024\n\t" \
  "v_mfma_f32_16x16x32_bf16 v[48:51], v[168:171], v[204:207], v[48:51]\n\t" \
  "global_load_lds_dwordx4 v222, s[36:37] offset:2048\n\t" \
  "global_load_lds_dwordx4 v223, s[36:37] offset:3072\n\t" \
  "s_add_u32 m0, m0, 4096\n\t" \
  "v_mfma_f32_16x16x32_bf16 v[52:55], v[172:175], v[204:207], v[52:55]\n\t" \
  "global_load_lds_dwordx4 v224, s[36:37]\n\t" \
  "s_add_u32 s36, s36, 40960\n\t" \
  "s_addc_u32 s37, s37, 0\n\t" \
  "v_mfma_f32_16x16x32_bf16 v[56:59], v[176:179], v[204:207], v[56:59]\n\t" \
  "global_load_dwordx4 v[204:207], v244, s[38:39] sc1\n\t" \
  "v_mfma_f32_16x16x32_bf16 v[60:63], v[160:163], v[208:211], v[60:63]\n\t" \
  "v_mfma_f32_16x16x32_bf16 v[64:67], v[164:167], v[208:211], v[64:67]\n\t" \
  "v_mfma_f32_16x16x32_bf16 v[68:71], v[168:171], v[208:211], v[68:71]\n\t" \
  "v_mfma_f32_16x16x32_bf16 v[72:75], v[172:175], v[208:211], v[72:75]\n\t" \
  "v_mfma_f32_16x16x32_bf16 v[76:79], v[176:179], v[208:211], v[76:79]\n\t" \
  "global_load_dwordx4 v[208:211], v246, s[38:39] sc1\n\t" \
  "s_add_u32 s38, s38, 1024\n\t" \
  "s_addc_u32 s39, s39, 0\n\t" \
  "s_waitcnt vmcnt(11)\n\t" \
  "s_sub_u32 s49, s49, 1\n\t" \
  "s_cmp_lg_u32 s49, 0\n\t" \
  "s_cbranch_scc1 .Lffn%=\n\t" \
  "ds_read_b64_tr_b16 v[160:161], v225 offset:5120\n\t" \
  "ds_read_b64_tr_b16 v[162:163], v225 offset:7680\n\t" \
  "ds_read_b64_tr_b16 v[164:165], v225 offset:5152\n\t" \
  "ds_read_b64_tr_b16 v[166:167], v225 offset:7712\n\t" \
  "ds_read_b64_tr_b16 v[168:169], v225 offset:5184\n\t" \
  "ds_read_b64_tr_b16 v[170:171], v225 offset:7744\n\t" \
  "ds_read_b64_tr_b16 v[172:173], v225 offset:5216\n\t" \
  "ds_read_b64_tr_b16 v[174:175], v225 offset:7776\n\t" \
  "ds_read_b64_tr_b16 v[176:177], v225 offset:5248\n\t" \
  "ds_read_b64_tr_b16 v[178:179], v225 offset:7808\n\t" \
  "s_waitcnt vmcnt(0) lgkmcnt(0)\n\t" \
  "s_mov_b32 s42, 4\n\t" \
  ".Lgb1%=:\n\t" \
  "s_setprio 2\n\t" \
  "s_barrier\n\t" \
  "s_branch .Lgb2%=\n\t" \
  "ds_read_b32 v248, v238 offset:8\n\t" \
  "s_waitcnt lgkmcnt(0)\n\t" \
  "v_cmp_gt_u32 vcc, s42, v248\n\t" \
  "s_nop 1\n\t" \
  "s_cbranch_vccz .Lgb2%=\n\t" \
  "s_sleep 1\n\t" \
  "s_branch .Lgb1%=\n\t" \
  ".Lgb2%=:\n\t" \
  "ds_read2st64_b32 v[212:213], v243 offset0:0 offset1:2\n\t" \
  "ds_read2st64_b32 v[214:215], v243 offset0:4 offset1:6\n\t" \
  "ds_read2st64_b32 v[216:217], v245 offset0:0 offset1:2\n\t" \
  "ds_read2st64_b32 v[218:219], v245 offset0:4 offset1:6\n\t" \
  "ds_read_b128 v[180:183], v241 offset:0\n\t" \
  "ds_read_b128 v[184:187], v241 offset:2560\n\t" \
  "ds_read_b128 v[188:191], v241 offset:5120\n\t" \
  "ds_read_b128 v[192:195], v241 offset:7680\n\t" \
  "ds_read_b128 v[196:199], v241 offset:64\n\t" \
  "ds_read_b128 v[200:203], v241 offset:2624\n\t" \
  "ds_read_b128 v[204:207], v241 offset:5184\n\t" \
  "ds_read_b128 v[208:211], v241 offset:7744\n\t" \
  "s_waitcnt lgkmcnt(8)\n\t" \
  "v_add_f32 v212, v212, v214\n\t" \
  "v_add_f32 v213, v213, v215\n\t" \
  "v_add_f32 v216, v216, v218\n\t" \
  "v_add_f32 v217, v217, v219\n\t" \
  "v_add_f32 v212, v212, v213\n\t" \
  "v_add_f32 v216, v216, v217\n\t" \
  "v_rcp_f32 v214, v212\n\t" \
  "v_rcp_f32 v218, v216\n\t" \
  "v_subrev_u32 v212, 0x21800000, v212\n\t" \
  "v_subrev_u32 v216, 0x21800000, v216\n\t" \
  "v_max_u32 v212, v212, v216\n\t" \
  "v_cmp_ge_u32 vcc, s43, v212\n\t" \
  "s_waitcnt lgkmcnt(4)\n\t" \
  "v_add_f32 v180, v180, v184\n\t" \
  "v_add_f32 v181, v181, v185\n\t" \
  "v_add_f32 v182, v182, v186\n\t" \
  "v_add_f32 v183, v183, v187\n\t" \
  "v_add_f32 v188, v188, v192\n\t" \
  "v_add_f32 v189, v189, v193\n\t" \
  "v_add_f32 v190, v190, v194\n\t" \
  "v_add_f32 v191, v191, v195\n\t" \
  "v_add_f32 v180, v180, v188\n\t" \
  "v_add_f32 v181, v181, v189\n\t" \
  "v_add_f32 v182, v182, v190\n\t" \
  "v_add_f32 v183, v183, v191\n\t" \
  "v_mul_f32 v180, v214, v180\n\t" \
  "v_mul_f32 v181, v214, v181\n\t" \
  "v_mul_f32 v182, v214, v182\n\t" \
  "v_mul_f32 v183, v214, v183\n\t" \
  "v_cvt_pk_bf16_f32 v184, v180, v181\n\t" \
  "v_cvt_pk_bf16_f32 v185, v182, v183\n\t" \
  "s_waitcnt lgkmcnt(0)\n\t" \
  "v_add_f32 v196, v196, v200\n\t" \
  "v_add_f32 v197, v197, v201\n\t" \
  "v_add_f32 v198, v198, v202\n\t" \
  "v_add_f32 v199, v199, v203\n\t" \
  "v_add_f32 v204, v204, v208\n\t" \
  "v_add_f32 v205, v205, v209\n\t" \
  "v_add_f32 v206, v206, v210\n\t" \
  "v_add_f32 v207, v207, v211\n\t" \
  "v_add_f32 v196, v196, v204\n\t" \
  "v_add_f32 v197, v197, v205\n\t" \
  "v_add_f32 v198, v198, v206\n\t" \
  "v_add_f32 v199, v199, v207\n\t" \
  "v_mul_f32 v196, v218, v196\n\t" \
  "v_mul_f32 v197, v218, v197\n\t" \
  "v_mul_f32 v198, v218, v198\n\t" \
  "v_mul_f32 v199, v218, v199\n\t" \
  "v_cvt_pk_bf16_f32 v186, v196, v197\n\t" \
  "v_cvt_pk_bf16_f32 v187, v198, v199\n\t" \
  "s_and_b64 exec, vcc, s[50:51]\n\t" \
  "global_store_dwordx4 v237, v[184:187], s[44:45] offset:0 sc1\n\t" \
  "s_mov_b64 exec, -1\n\t" \
  "s_andn2_b64 s[50:51], s[50:51], vcc\n\t" \
  "s_cmp_eq_u32 s34, 3\n\t" \
  "s_cbranch_scc0 .Lgc2%=\n\t" \
  "ds_read2st64_b32 v[212:213], v249 offset0:0 offset1:2\n\t" \
  "ds_read2st64_b32 v[214:215], v249 offset0:4 offset1:6\n\t" \
  "ds_read2st64_b32 v[216:217], v251 offset0:0 offset1:2\n\t" \
  "ds_read2st64_b32 v[218:219], v251 offset0:4 offset1:6\n\t" \
  "ds_read_b128 v[180:183], v247 offset:0\n\t" \
  "ds_read_b128 v[184:187], v247 offset:2560\n\t" \
  "ds_read_b128 v[188:191], v247 offset:5120\n\t" \
  "ds_read_b128 v[192:195], v247 offset:7680\n\t" \
  "ds_read_b128 v[196:199], v247 offset:64\n\t" \
  "ds_read_b128 v[200:203], v247 offset:2624\n\t" \
  "ds_read_b128 v[204:207], v247 offset:5184\n\t" \
  "ds_read_b128 v[208:211], v247 offset:7744\n\t" \
  "s_waitcnt lgkmcnt(8)\n\t" \
  "v_add_f32 v212, v212, v214\n\t" \
  "v_add_f32 v213, v213, v215\n\t" \
  "v_add_f32 v216, v216, v218\n\t" \
  "v_add_f32 v217, v217, v219\n\t" \
  "v_add_f32 v212, v212, v213\n\t" \
  "v_add_f32 v216, v216, v217\n\t" \
  "v_rcp_f32 v214, v212\n\t" \
  "v_rcp_f32 v218, v216\n\t" \
  "v_subrev_u32 v212, 0x21800000, v212\n\t" \
  "v_subrev_u32 v216, 0x21800000, v216\n\t" \
  "v_max_u32 v212, v212, v216\n\t" \
  "v_cmp_ge_u32 vcc, s43, v212\n\t" \
  "s_waitcnt lgkmcnt(4)\n\t" \
  "v_add_f32 v180, v180, v184\n\t" \
  "v_add_f32 v181, v181, v185\n\t" \
  "v_add_f32 v182, v182, v186\n\t" \
  "v_add_f32 v183, v183, v187\n\t" \
  "v_add_f32 v188, v188, v192\n\t" \
  "v_add_f32 v189, v189, v193\n\t" \
  "v_add_f32 v190, v190, v194\n\t" \
  "v_add_f32 v191, v191, v195\n\t" \
  "v_add_f32 v180, v180, v188\n\t" \
  "v_add_f32 v181, v181, v189\n\t" \
  "v_add_f32 v182, v182, v190\n\t" \
  "v_add_f32 v183, v183, v191\n\t" \
  "v_mul_f32 v180, v214, v180\n\t" \
  "v_mul_f32 v181, v214, v181\n\t" \
  "v_mul_f32 v182, v214, v182\n\t" \
  "v_mul_f32 v183, v214, v183\n\t" \
  "v_cvt_pk_bf16_f32 v184, v180, v181\n\t" \
  "v_cvt_pk_bf16_f32 v185, v182, v183\n\t" \
  "s_waitcnt lgkmcnt(0)\n\t" \
  "v_add_f32 v196, v196, v200\n\t" \
  "v_add_f32 v197, v197, v201\n\t" \
  "v_add_f32 v198, v198, v202\n\t" \
  "v_add_f32 v199, v199, v203\n\t" \
  "v_add_f32 v204, v204, v208\n\t" \
  "v_add_f32 v205, v205, v209\n\t" \
  "v_add_f32 v206, v206, v210\n\t" \
  "v_add_f32 v207, v207, v211\n\t" \
  "v_add_f32 v196, v196, v204\n\t" \
  "v_add_f32 v197, v197, v205\n\t" \
  "v_add_f32 v198, v198, v206\n\t" \
  "v_add_f32 v199, v199, v207\n\t" \
  "v_mul_f32 v196, v218, v196\n\t" \
  "v_mul_f32 v197, v218, v197\n\t" \
  "v_mul_f32 v198, v218, v198\n\t" \
  "v_mul_f32 v199, v218, v199\n\t" \
  "v_cvt_pk_bf16_f32 v186, v196, v197\n\t" \
  "v_cvt_pk_bf16_f32 v187, v198, v199\n\t" \
  "s_and_b64 exec, vcc, s[52:53]\n\t" \
  "global_store_dwordx4 v237, v[184:187], s[44:45] offset:1024 sc1\n\t" \
  "s_mov_b64 exec, -1\n\t" \
  "s_andn2_b64 s[52:53], s[52:53], vcc\n\t" \
  "s_cbranch_scc1 .Lgbad%=\n\t" \
  ".Lgc2%=:\n\t" \
  "s_cmp_eq_u64 s[50:51], 0\n\t" \
  "s_cbranch_scc1 .Lgok%=\n\t" \
  ".Lgbad%=:\n\t" \
  "v_mov_b32 v248, 1\n\t" \
  "ds_write_b32 v238, v248\n\t" \
  "s_waitcnt lgkmcnt(0)\n\t" \
  ".Lgok%=:\n\t" \
  "s_setprio 0\n\t" \
  "s_barrier\n\t" \
  "s_waitcnt vmcnt(0)\n\t" \
  "global_load_dwordx4 v[180:183], v227, s[46:47] offset:-3072 sc1\n\t" \
  "global_load_dwordx4 v[184:187], v227, s[46:47] offset:-2816 sc1\n\t" \
  "global_load_dwordx4 v[188:191], v227, s[46:47] offset:-2560 sc1\n\t" \
  "global_load_dwordx4 v[192:195], v227, s[46:47] offset:-2304 sc1\n\t" \
  "global_load_dwordx4 v[196:199], v227, s[46:47] offset:-2048 sc1\n\t" \
  "global_load_dwordx4 v[200:203], v236, s[46:47] offset:-3072 sc1\n\t" \
  "global_load_dwordx4 v[204:207], v236, s[46:47] offset:-2816 sc1\n\t" \
  "global_load_dwordx4 v[208:211], v236, s[46:47] offset:-2560 sc1\n\t" \
  "global_load_dwordx4 v[212:215], v236, s[46:47] offset:-2304 sc1\n\t" \
  "global_load_dwordx4 v[216:219], v236, s[46:47] offset:-2048 sc1\n\t" \
  "s_mov_b32 s64, 0x100000\n\t" \
  "s_barrier\n\t" \
  "ds_read_b32 v248, v238\n\t" \
  "s_waitcnt lgkmcnt(0)\n\t" \
  "v_readfirstlane_b32 s63, v248\n\t" \
  "s_nop 1\n\t" \
  "s_andn2_b32 s63, s63, s43\n\t" \
  "s_cbranch_scc0 .Lgl0%=\n\t" \
  "s_mov_b32 s43, -1\n\t" \
  "s_mov_b32 s42, 8\n\t" \
  "s_waitcnt vmcnt(0)\n\t" \
  "s_barrier\n\t" \
  "s_barrier\n\t" \
  "s_branch .Lgb1%=\n\t" \
  ".Lgl0%=:\n\t" \
  "s_waitcnt vmcnt(5)\n\t" \
  "v_max3_u32 v254, v180, v181, v182\n\t" \
  "v_max3_u32 v255, v183, v184, v185\n\t" \
  "v_max3_u32 v254, v254, v186, v187\n\t" \
  "v_max3_u32 v255, v255, v188, v189\n\t" \
  "v_max3_u32 v254, v254, v190, v191\n\t" \
  "v_max3_u32 v255, v255, v192, v193\n\t" \
  "v_max3_u32 v254, v254, v194, v195\n\t" \
  "v_max3_u32 v255, v255, v196, v197\n\t" \
  "v_max3_u32 v254, v254, v198, v199\n\t" \
  "v_max_u32 v254, v254, v255\n\t" \
  "v_cmp_eq_u32 vcc, -1, v254\n\t" \
  "s_nop 1\n\t" \
  "s_cbranch_vccnz .Lgr0%=\n\t" \
  ".Lgm0%=:\n\t" \
  "v_mfma_f32_16x16x32_bf16 v[0:3], v[80:83], v[180:183], v[0:3]\n\t" \
  "v_mfma_f32_16x16x32_bf16 v[4:7], v[84:87], v[180:183], v[4:7]\n\t" \
  "v_mfma_f32_16x16x32_bf16 v[8:11], v[88:91], v[180:183], v[8:11]\n\t" \
  "v_mfma_f32_16x16x32_bf16 v[12:15], v[92:95], v[180:183], v[12:15]\n\t" \
  "v_mfma_f32_16x16x32_bf16 v[16:19], v[96:99], v[180:183], v[16:19]\n\t" \
  "v_mfma_f32_16x16x32_bf16 v[0:3], v[100:103], v[184:187], v[0:3]\n\t" \
  "v_mfma_f32_16x16x32_bf16 v[4:7], v[104:107], v[184:187], v[4:7]\n\t" \
  "v_mfma_f32_16x16x32_bf16 v[8:11], v[108:111], v[184:187], v[8:11]\n\t" \
  "v_mfma_f32_16x16x32_bf16 v[12:15], v[112:115], v[184:187], v[12:15]\n\t" \
  "v_mfma_f32_16x16x32_bf16 v[16:19], v[116:119], v[184:187], v[16:19]\n\t" \
  "v_mfma_f32_16x16x32_bf16 v[0:3], v[120:123], v[188:191], v[0:3]\n\t" \
  "v_mfma_f32_16x16x32_bf16 v[4:7], v[124:127], v[188:191], v[4:7]\n\t" \
  "v_mfma_f32_16x16x32_bf16 v[8:11], v[128:131], v[188:191], v[8:11]\n\t" \
  "v_mfma_f32_16x16x32_bf16 v[12:15], v[132:135], v[188:191], v[12:15]\n\t" \
  "v_mfma_f32_16x16x32_bf16 v[16:19], v[136:139], v[188:191], v[16:19]\n\t" \
  "v_mfma_f32_16x16x32_bf16 v[0:3], v[140:143], v[192:195], v[0:3]\n\t" \
  "v_mfma_f32_16x16x32_bf16 v[4:7], v[144:147], v[192:195], v[4:7]\n\t" \
  "v_mfma_f32_16x16x32_bf16 v[8:11], v[148:151], v[192:195], v[8:11]\n\t" \
  "s_waitcnt vmcnt(0)\n\t" \
  "v_max3_u32 v254, v200, v201, v202\n\t" \
  "v_mfma_f32_16x16x32_bf16 v[12:15], v[152:155], v[192:195], v[12:15]\n\t" \
  "v_max3_u32 v255, v203, v204, v205\n\t" \
  "v_max3_u32 v254, v254, v206, v207\n\t" \
  "v_mfma_f32_16x16x32_bf16 v[16:19], v[156:159], v[192:195], v[16:19]\n\t" \
  "v_max3_u32 v255, v255, v208, v209\n\t" \
  "v_max3_u32 v254, v254, v210, v211\n\t" \
  "v_mfma_f32_16x16x32_bf16 v[0:3], v[160:163], v[196:199], v[0:3]\n\t" \
  "v_max3_u32 v255, v255, v212, v213\n\t" \
  "v_max3_u32 v254, v254, v214, v215\n\t" \
  "v_mfma_f32_16x16x32_bf16 v[4:7], v[164:167], v[196:199], v[4:7]\n\t" \
  "v_max3_u32 v255, v255, v216, v217\n\t" \
  "v_max3_u32 v254, v254, v218, v219\n\t" \
  "v_mfma_f32_16x16x32_bf16 v[8:11], v[168:171], v[196:199], v[8:11]\n\t" \
  "v_max_u32 v254, v254, v255\n\t" \
  "v_cmp_eq_u32 vcc, -1, v254\n\t" \
  "v_mfma_f32_16x16x32_bf16 v[12:15], v[172:175], v[196:199], v[12:15]\n\t" \
  "v_mfma_f32_16x16x32_bf16 v[16:19], v[176:179], v[196:199], v[16:19]\n\t" \
  "s_cbranch_vccnz .Lgr1%=\n\t" \
  ".Lgm1%=:\n\t" \
  "v_mfma_f32_16x16x32_bf16 v[20:23], v[80:83], v[200:203], v[20:23]\n\t" \
  "v_mfma_f32_16x16x32_bf16 v[24:27], v[84:87], v[200:203], v[24:27]\n\t" \
  "v_mfma_f32_16x16x32_bf16 v[28:31], v[88:91], v[200:203], v[28:31]\n\t" \
  "v_mfma_f32_16x16x32_bf16 v[32:35], v[92:95], v[200:203], v[32:35]\n\t" \
  "ds_write_b128 v226, v[0:3] offset:0\n\t" \
  "v_mfma_f32_16x16x32_bf16 v[36:39], v[96:99], v[200:203], v[36:39]\n\t" \
  "ds_write_b128 v226, v[4:7] offset:64\n\t" \
  "v_mfma_f32_16x16x32_bf16 v[20:23], v[100:103], v[204:207], v[20:23]\n\t" \
  "ds_write_b128 v226, v[8:11] offset:128\n\t" \
  "v_mfma_f32_16x16x32_bf16 v[24:27], v[104:107], v[204:207], v[24:27]\n\t" \
  "ds_write_b128 v226, v[12:15] offset:192\n\t" \
  "v_mfma_f32_16x16x32_bf16 v[28:31], v[108:111], v[204:207], v[28:31]\n\t" \
  "ds_write_b128 v226, v[16:19] offset:256\n\t" \
  "v_mfma_f32_16x16x32_bf16 v[32:35], v[112:115], v[204:207], v[32:35]\n\t" \
  "v_mfma_f32_16x16x32_bf16 v[36:39], v[116:119], v[204:207], v[36:39]\n\t" \
  "v_mfma_f32_16x16x32_bf16 v[20:23], v[120:123], v[208:211], v[20:23]\n\t" \
  "v_mfma_f32_16x16x32_bf16 v[24:27], v[124:127], v[208:211], v[24:27]\n\t" \
  "v_mfma_f32_16x16x32_bf16 v[28:31], v[128:131], v[208:211], v[28:31]\n\t" \
  "s_waitcnt lgkmcnt(0)\n\t" \
  "s_barrier\n\t" \
  "global_load_dwordx4 v[180:183], v227, s[46:47] offset:2048 sc1\n\t" \
  "global_load_dwordx4 v[184:187], v227, s[46:47] offset:2304 sc1\n\t" \
  "global_load_dwordx4 v[188:191], v227, s[46:47] offset:2560 sc1\n\t" \
  "global_load_dwordx4 v[192:195], v227, s[46:47] offset:2816 sc1\n\t" \
  "global_load_dwordx4 v[196:199], v227, s[46:47] offset:3072 sc1\n\t" \
  "v_mfma_f32_16x16x32_bf16 v[32:35], v[132:135], v[208:211], v[32:35]\n\t" \
  "v_mfma_f32_16x16x32_bf16 v[36:39], v[136:139], v[208:211], v[36:39]\n\t" \
  "v_mfma_f32_16x16x32_bf16 v[20:23], v[140:143], v[212:215], v[20:23]\n\t" \
  "v_mfma_f32_16x16x32_bf16 v[24:27], v[144:147], v[212:215], v[24:27]\n\t" \
  "v_mfma_f32_16x16x32_bf16 v[28:31], v[148:151], v[212:215], v[28:31]\n\t" \
  "v_mfma_f32_16x16x32_bf16 v[32:35], v[152:155], v[212:215], v[32:35]\n\t" \
  "v_mfma_f32_16x16x32_bf16 v[36:39], v[156:159], v[212:215], v[36:39]\n\t" \
  "v_mfma_f32_16x16x32_bf16 v[20:23], v[160:163], v[216:219], v[20:23]\n\t" \
  "v_mfma_f32_16x16x32_bf16 v[24:27], v[164:167], v[216:219], v[24:27]\n\t" \
  "v_mfma_f32_16x16x32_bf16 v[28:31], v[168:171], v[216:219], v[28:31]\n\t" \
  "v_mfma_f32_16x16x32_bf16 v[32:35], v[172:175], v[216:219], v[32:35]\n\t" \
  "v_mfma_f32_16x16x32_bf16 v[36:39], v[176:179], v[216:219], v[36:39]\n\t" \
  "global_load_dwordx4 v[200:203], v236, s[46:47] offset:2048 sc1\n\t" \
  "global_load_dwordx4 v[204:207], v236, s[46:47] offset:2304 sc1\n\t" \
  "global_load_dwordx4 v[208:211], v236, s[46:47] offset:2560 sc1\n\t" \
  "global_load_dwordx4 v[212:215], v236, s[46:47] offset:2816 sc1\n\t" \
  "global_load_dwordx4 v[216:219], v236, s[46:47] offset:3072 sc1\n\t" \
  "ds_write_b128 v226, v[20:23] offset:5376\n\t" \
  "ds_write_b128 v226, v[24:27] offset:5440\n\t" \
  "ds_write_b128 v226, v[28:31] offset:5504\n\t" \
  "ds_write_b128 v226, v[32:35] offset:5568\n\t" \
  "ds_write_b128 v226, v[36:39] offset:5632\n\t" \
  "s_waitcnt lgkmcnt(0)\n\t" \
  "s_barrier\n\t" \
  "s_waitcnt vmcnt(5)\n\t" \
  "v_max3_u32 v254, v180, v181, v182\n\t" \
  "v_max3_u32 v255, v183, v184, v185\n\t" \
  "v_max3_u32 v254, v254, v186, v187\n\t" \
  "v_max3_u32 v255, v255, v188, v189\n\t" \
  "v_max3_u32 v254, v254, v190, v191\n\t" \
  "v_max3_u32 v255, v255, v192, v193\n\t" \
  "v_max3_u32 v254, v254, v194, v195\n\t" \
  "v_max3_u32 v255, v255, v196, v197\n\t" \
  "v_max3_u32 v254, v254, v198, v199\n\t" \
  "v_max_u32 v254, v254, v255\n\t" \
  "v_cmp_eq_u32 vcc, -1, v254\n\t" \
  "s_nop 1\n\t" \
  "s_cbranch_vccnz .Lgr2%=\n\t" \
  ".Lgm2%=:\n\t" \
  "v_mfma_f32_16x16x32_bf16 v[40:43], v[80:83], v[180:183], v[40:43]\n\t" \
  "v_mfma_f32_16x16x32_bf16 v[44:47], v[84:87], v[180:183], v[44:47]\n\t" \
  "v_mfma_f32_16x16x32_bf16 v[48:51], v[88:91], v[180:183], v[48:51]\n\t" \
  "v_mfma_f32_16x16x32_bf16 v[52:55], v[92:95], v[180:183], v[52:55]\n\t" \
  "v_mfma_f32_16x16x32_bf16 v[56:59], v[96:99], v[180:183], v[56:59]\n\t" \
  "v_mfma_f32_16x16x32_bf16 v[40:43], v[100:103], v[184:187], v[40:43]\n\t" \
  "v_mfma_f32_16x16x32_bf16 v[44:47], v[104:107], v[184:187], v[44:47]\n\t" \
  "v_mfma_f32_16x16x32_bf16 v[48:51], v[108:111], v[184:187], v[48:51]\n\t" \
  "v_mfma_f32_16x16x32_bf16 v[52:55], v[112:115], v[184:187], v[52:55]\n\t" \
  "v_mfma_f32_16x16x32_bf16 v[56:59], v[116:119], v[184:187], v[56:59]\n\t" \
  "v_mfma_f32_16x16x32_bf16 v[40:43], v[120:123], v[188:191], v[40:43]\n\t" \
  "v_mfma_f32_16x16x32_bf16 v[44:47], v[124:127], v[188:191], v[44:47]\n\t" \
  "v_mfma_f32_16x16x32_bf16 v[48:51], v[128:131], v[188:191], v[48:51]\n\t" \
  "v_mfma_f32_16x16x32_bf16 v[52:55], v[132:135], v[188:191], v[52:55]\n\t" \
  "v_mfma_f32_16x16x32_bf16 v[56:59], v[136:139], v[188:191], v[56:59]\n\t" \
  "v_mfma_f32_16x16x32_bf16 v[40:43], v[140:143], v[192:195], v[40:43]\n\t" \
  "v_mfma_f32_16x16x32_bf16 v[44:47], v[144:147], v[192:195], v[44:47]\n\t" \
  "v_mfma_f32_16x16x32_bf16 v[48:51], v[148:151], v[192:195], v[48:51]\n\t" \
  "s_waitcnt vmcnt(0)\n\t" \
  "v_max3_u32 v254, v200, v201, v202\n\t" \
  "v_mfma_f32_16x16x32_bf16 v[52:55], v[152:155], v[192:195], v[52:55]\n\t" \
  "v_max3_u32 v255, v203, v204, v205\n\t" \
  "v_max3_u32 v254, v254, v206, v207\n\t" \
  "v_mfma_f32_16x16x32_bf16 v[56:59], v[156:159], v[192:195], v[56:59]\n\t" \
  "v_max3_u32 v255, v255, v208, v209\n\t" \
  "v_max3_u32 v254, v254, v210, v211\n\t" \
  "v_mfma_f32_16x16x32_bf16 v[40:43], v[160:163], v[196:199], v[40:43]\n\t" \
  "v_max3_u32 v255, v255, v212, v213\n\t" \
  "v_max3_u32 v254, v254, v214, v215\n\t" \
  "v_mfma_f32_16x16x32_bf16 v[44:47], v[164:167], v[196:199], v[44:47]\n\t" \
  "v_max3_u32 v255, v255, v216, v217\n\t" \
  "v_max3_u32 v254, v254, v218, v219\n\t" \
  "v_mfma_f32_16x16x32_bf16 v[48:51], v[168:171], v[196:199], v[48:51]\n\t" \
  "v_max_u32 v254, v254, v255\n\t" \
  "v_cmp_eq_u32 vcc, -1, v254\n\t" \
  "v_mfma_f32_16x16x32_bf16 v[52:55], v[172:175], v[196:199], v[52:55]\n\t" \
  "v_mfma_f32_16x16x32_bf16 v[56:59], v[176:179], v[196:199], v[56:59]\n\t" \
  "s_cbranch_vccnz .Lgr3%=\n\t" \
  ".Lgm3%=:\n\t" \
  "v_mfma_f32_16x16x32_bf16 v[60:63], v[80:83], v[200:203], v[60:63]\n\t" \
  "v_mfma_f32_16x16x32_bf16 v[64:67], v[84:87], v[200:203], v[64:67]\n\t" \
  "v_mfma_f32_16x16x32_bf16 v[68:71], v[88:91], v[200:203], v[68:71]\n\t" \
  "v_mfma_f32_16x16x32_bf16 v[72:75], v[92:95], v[200:203], v[72:75]\n\t" \
  "ds_write_b128 v226, v[40:43] offset:10752\n\t" \
  "v_mfma_f32_16x16x32_bf16 v[76:79], v[96:99], v[200:203], v[76:79]\n\t" \
  "ds_write_b128 v226, v[44:47] offset:10816\n\t" \
  "v_mfma_f32_16x16x32_bf16 v[60:63], v[100:103], v[204:207], v[60:63]\n\t" \
  "ds_write_b128 v226, v[48:51] offset:10880\n\t" \
  "v_mfma_f32_16x16x32_bf16 v[64:67], v[104:107], v[204:207], v[64:67]\n\t" \
  "ds_write_b128 v226, v[52:55] offset:10944\n\t" \
  "v_mfma_f32_16x16x32_bf16 v[68:71], v[108:111], v[204:207], v[68:71]\n\t" \
  "ds_write_b128 v226, v[56:59] offset:11008\n\t" \
  "v_mfma_f32_16x16x32_bf16 v[72:75], v[112:115], v[204:207], v[72:75]\n\t" \
  "v_mfma_f32_16x16x32_bf16 v[76:79], v[116:119], v[204:207], v[76:79]\n\t" \
  "v_mfma_f32_16x16x32_bf16 v[60:63], v[120:123], v[208:211], v[60:63]\n\t" \
  "v_mfma_f32_16x16x32_bf16 v[64:67], v[124:127], v[208:211], v[64:67]\n\t" \
  "v_mfma_f32_16x16x32_bf16 v[68:71], v[128:131], v[208:211], v[68:71]\n\t" \
  "s_waitcnt lgkmcnt(0)\n\t" \
  "s_barrier\n\t" \
  "v_mfma_f32_16x16x32_bf16 v[72:75], v[132:135], v[208:211], v[72:75]\n\t" \
  "v_mfma_f32_16x16x32_bf16 v[76:79], v[136:139], v[208:211], v[76:79]\n\t" \
  "v_mfma_f32_16x16x32_bf16 v[60:63], v[140:143], v[212:215], v[60:63]\n\t" \
  "v_mfma_f32_16x16x32_bf16 v[64:67], v[144:147], v[212:215], v[64:67]\n\t" \
  "v_mfma_f32_16x16x32_bf16 v[68:71], v[148:151], v[212:215], v[68:71]\n\t" \
  "v_mfma_f32_16x16x32_bf16 v[72:75], v[152:155], v[212:215], v[72:75]\n\t" \
  "v_mfma_f32_16x16x32_bf16 v[76:79], v[156:159], v[212:215], v[76:79]\n\t" \
  "v_mfma_f32_16x16x32_bf16 v[60:63], v[160:163], v[216:219], v[60:63]\n\t" \
  "v_mfma_f32_16x16x32_bf16 v[64:67], v[164:167], v[216:219], v[64:67]\n\t" \
  "v_mfma_f32_16x16x32_bf16 v[68:71], v[168:171], v[216:219], v[68:71]\n\t" \
  "v_mfma_f32_16x16x32_bf16 v[72:75], v[172:175], v[216:219], v[72:75]\n\t" \
  "v_mfma_f32_16x16x32_bf16 v[76:79], v[176:179], v[216:219], v[76:79]\n\t" \
  "s_nop 3\n\t" \
  "ds_write_b128 v226, v[60:63] offset:16128\n\t" \
  "ds_write_b128 v226, v[64:67] offset:16192\n\t" \
  "ds_write_b128 v226, v[68:71] offset:16256\n\t" \
  "ds_write_b128 v226, v[72:75] offset:16320\n\t" \
  "ds_write_b128 v226, v[76:79] offset:16384\n\t" \
  "s_waitcnt lgkmcnt(0)\n\t" \
  "s_barrier\n\t" \
  ".Lend%=:\n\t" \
  "s_endpgm\n\t" \
  ""

struct Params { const void* x; const void* wq; const void* wk; const void* wv; const void* wo; const void* wg; const void* wu; const void* wd; void* ws; void* out; };

__global__ __launch_bounds__(512, 2) void k_fused(Params p) {
  asm volatile(KERNEL_ASM :: [ka] "{s[0:1]}"(__builtin_amdgcn_kernarg_segment_ptr()), [bid] "{s2}"(blockIdx.x), "{v0}"(threadIdx.x) : "v255", "s99", "vcc", "scc", "memory");
}

extern "C" void kernel_launch(void* const* d_in, const int* in_sizes, int n_in, void* d_out, int out_size, void* d_ws, size_t ws_size,
                              hipStream_t stream) {
  (void)hipFuncSetAttribute((const void*)k_fused, hipFuncAttributeMaxDynamicSharedMemorySize, 163840);
  Params p{d_in[0], d_in[1], d_in[2], d_in[3], d_in[4], d_in[6], d_in[7], d_in[8], d_ws, d_out};
  k_fused<<<256, 512, 163840, stream>>>(p);
}
```
